```python
import torch

def _softmax(x, axis=-1):
    return torch.softmax(x, dim=axis)

def _gelu(x):
    return torch.nn.functional.gelu(x, approximate='tanh')

def _silu(x):
    return torch.nn.functional.silu(x)

def _sigmoid(x):
    return torch.sigmoid(x)

def _layernorm(x, axis=-1, eps=1e-05):
    m = x.mean(dim=axis, keepdim=True)
    v = x.var(dim=axis, keepdim=True, unbiased=False)
    return (x - m) / torch.sqrt(v + eps)

def _scatter_add(tgt, idx, src, axis=0):
    return tgt.clone().index_add_(axis, idx, src)

def _segment_sum(src, seg, n):
    out = torch.zeros((n,) + src.shape[1:], dtype=src.dtype)
    return out.index_add_(0, seg, src)

def _pad(x, pw):
    flat = tuple((v for pair in reversed(pw) for v in pair))
    return torch.nn.functional.pad(x, flat)
kernel_name = 'standalone__tree_speculative_verify_tree_attention_over_candidate_token_tree__prompt'

def setup_inputs():

    _w = lambda a: (lambda t: t.to(torch.bfloat16) if t.is_floating_point() else t)(torch.as_tensor(a))
    B, Hq, Hkv, T, C, D = (1, 32, 8, 2048, 2048, 128)
    tree_mask = torch.zeros((B, T, T), dtype=float)
    for b in range(B):
        parent = torch.zeros(T, dtype=torch.int64)
        for i in range(1, T):
            parent[i] = int(torch.randint(0, i, (1,)).item())
        for i in range(T):
            j = i
            tree_mask[b, i, j] = 1.0
            while j > 0:
                j = parent[j]
                tree_mask[b, i, j] = 1.0
    return {'q': _w((lambda *s: torch.randn(*s, dtype=torch.bfloat16) * 0.8)(B, Hq, T, D)), 'k_cache': _w((lambda *s: torch.randn(*s, dtype=torch.bfloat16) * 0.8)(B, Hkv, C, D)), 'v_cache': _w((lambda *s: torch.randn(*s, dtype=torch.bfloat16) * 0.05)(B, Hkv, C, D)), 'k_tree': _w((lambda *s: torch.randn(*s, dtype=torch.bfloat16) * 0.8)(B, Hkv, T, D)), 'v_tree': _w((lambda *s: torch.randn(*s, dtype=torch.bfloat16) * 0.05)(B, Hkv, T, D)), 'tree_mask': _w(tree_mask)}

def reference(q, k_cache, v_cache, k_tree, v_tree, tree_mask):
    B, Hq, T, D = q.shape
    Hkv, C = (k_cache.shape[1], k_cache.shape[2])
    rep = Hq // Hkv
    kc = (lambda x, n, axis: torch.repeat_interleave(x, n, dim=axis))(k_cache, rep, axis=1)
    vc = (lambda x, n, axis: torch.repeat_interleave(x, n, dim=axis))(v_cache, rep, axis=1)
    kt = (lambda x, n, axis: torch.repeat_interleave(x, n, dim=axis))(k_tree, rep, axis=1)
    vt = (lambda x, n, axis: torch.repeat_interleave(x, n, dim=axis))(v_tree, rep, axis=1)
    k = torch.concatenate([kc, kt], axis=2)
    v = torch.concatenate([vc, vt], axis=2)
    scale = D ** (-0.5)
    scores = torch.einsum('bhqd,bhkd->bhqk', q, k) * scale
    ctx_mask = torch.ones((B, 1, T, C), dtype=scores.dtype)
    tm = tree_mask[:, None, :, :].clone()
    full_mask = torch.concatenate([ctx_mask, tm], axis=-1)
    scores = scores + (1.0 - full_mask) * -1000000000.0
    p = _softmax(scores, axis=-1)
    return torch.einsum('bhqk,bhkd->bhqd', p, v)
```

```cpp
#include <hip/hip_runtime.h>
#include <hip/hip_bf16.h>
#include <stdint.h>
#include <utility>

constexpr int D = 128, HQ = 32, HKV = 8, T = 2048, C = 2048, NT = C / 64;
constexpr float THR = 8.0f;
constexpr unsigned KRS = 256u, KTILEB = 64u * KRS;
constexpr unsigned REC_CAP = 8u * 2048u;
constexpr unsigned REC_INVALID = 0x7800u;

typedef float    f32x16 __attribute__((ext_vector_type(16)));
typedef float    f32x2  __attribute__((ext_vector_type(2)));
typedef float    f32x4  __attribute__((ext_vector_type(4)));
typedef __bf16   bf16x8 __attribute__((ext_vector_type(8)));
typedef __bf16   bf16x4 __attribute__((ext_vector_type(4)));
typedef __bf16   bf16x2 __attribute__((ext_vector_type(2)));
typedef unsigned u32x4  __attribute__((ext_vector_type(4)));
typedef unsigned u32x2  __attribute__((ext_vector_type(2)));
template <int... Is> using iseq = std::integer_sequence<int, Is...>;
template <int N> using make_iseq = std::make_integer_sequence<int, N>;

#define KRING 0x0000u
#define VRING 0x8000u
#define TREEV 0x10000u
#define TREEREC 0x18000u
#define LDS_BYTES 0x18200u
#define SRD_WORD3 0x00020000
#define DI __device__ __forceinline__
#define GAP() __builtin_amdgcn_sched_barrier(0)
#define W4_ACLOB_HI "a128","a129","a130","a131","a132","a133","a134","a135","a136","a137","a138","a139","a140","a141","a142","a143","a144","a145","a146","a147","a148","a149","a150","a151","a152","a153","a154","a155","a156","a157","a158","a159","a160","a161","a162","a163","a164","a165","a166","a167","a168","a169","a170","a171","a172","a173","a174","a175","a176","a177","a178","a179","a180","a181","a182","a183","a184","a185","a186","a187","a188","a189","a190","a191","a192","a193","a194","a195","a196","a197","a198","a199","a200","a201","a202","a203","a204","a205","a206","a207","a208","a209","a210","a211","a212","a213","a214","a215","a216","a217","a218","a219","a220","a221","a222","a223","a224","a225","a226","a227","a228","a229","a230","a231","a232","a233","a234","a235","a236","a237","a238","a239","a240","a241","a242","a243","a244","a245","a246","a247","a248","a249","a250","a251","a252","a253","a254","a255"
#define W4_ACLOB "a0","a1","a2","a3","a4","a5","a6","a7","a8","a9","a10","a11","a12","a13","a14","a15","a16","a17","a18","a19","a20","a21","a22","a23","a24","a25","a26","a27","a28","a29","a30","a31","a32","a33","a34","a35","a36","a37","a38","a39","a40","a41","a42","a43","a44","a45","a46","a47","a48","a49","a50","a51","a52","a53","a54","a55","a56","a57","a58","a59","a60","a61","a62","a63","a64","a65","a66","a67","a68","a69","a70","a71","a72","a73","a74","a75","a76","a77","a78","a79","a80","a81","a82","a83","a84","a85","a86","a87","a88","a89","a90","a91","a92","a93","a94","a95","a96","a97","a98","a99","a100","a101","a102","a103","a104","a105","a106","a107","a108","a109","a110","a111","a112","a113","a114","a115","a116","a117","a118","a119","a120","a121","a122","a123","a124","a125","a126","a127"

DI unsigned rfl(unsigned x) { return __builtin_amdgcn_readfirstlane(x); }
DI unsigned cvtpk(float lo, float hi) {
  f32x2 v = {lo, hi}; bf16x2 b = __builtin_convertvector(v, bf16x2); return __builtin_bit_cast(unsigned, b);
}
DI float max3f(float a, float b, float c) { float r; asm("v_max3_f32 %0, %1, %2, %3" : "=v"(r) : "v"(a), "v"(b), "v"(c)); return r; }
DI float max2f(float a, float b) { float r; asm("v_max_f32_e32 %0, %1, %2" : "=v"(r) : "v"(a), "v"(b)); return r; }
DI float fadd_s(float a, float b) { float r; asm("v_add_f32_e32 %0, %1, %2" : "=v"(r) : "v"(a), "v"(b)); return r; }
DI float fmul_s(float a, float b) { float r; asm("v_mul_f32_e32 %0, %1, %2" : "=v"(r) : "v"(a), "v"(b)); return r; }
DI float fsub_s(float a, float b) { float r; asm("v_sub_f32_e32 %0, %1, %2" : "=v"(r) : "v"(a), "v"(b)); return r; }
DI float w4_aone() { unsigned l; asm volatile("v_mbcnt_lo_u32_b32 %0, -1, 0\n\tv_mbcnt_hi_u32_b32 %0, -1, %0" : "=v"(l)); return l < 32u ? 1.f : 0.f; }
DI void negm_bcast(f32x16& negm, float aone, float neg_mhat) {
  asm volatile("s_nop 1\n\tv_mfma_f32_32x32x2_f32 %0, %1, %2, 0" : "+v"(negm) : "v"(aone), "v"(neg_mhat));
}

struct W4C {
  unsigned vkb[4], vvb, vksrc[2], vvsrc, m0k, m0v, sofk, sofv, ldsb;
  __amdgpu_buffer_rsrc_t srdK, srdV;
  float THRv;
  f32x16 S[2][2][2];
  f32x16 NEGM[2];
  bf16x8 Vf[4][4];
  bf16x8 Pk[2][4];
  bf16x4 vlo[4][4], vup[4][4];
  float mhat[2], lsum[2], fct[2]; bool pend;
  float rs0[2], rs1[2], pmx0[2], pmx1[2], rmax[2];
  unsigned pm0[8], psf[8];
};

constexpr int AO_(int db, int qb) { return 16 * (2 * db + qb); }
constexpr int AQ_(int qb, int d0) { return 128 + 32 * qb + 4 * d0; }
constexpr int AK_(int kb, int d0) { return 192 + 32 * kb + 4 * d0; }

template <int I> DI void tr_read(W4C& c, unsigned vslot) {
  constexpr int ks = I >> 3, db = (I >> 1) & 3;
  if constexpr ((I & 1) == 0) asm volatile("ds_read_b64_tr_b16 %0, %1 offset:%2" : "=v"(c.vlo[db][ks]) : "v"(c.vvb + vslot), "i"(4096 * ks + 512 * db) : "memory");
  else                        asm volatile("ds_read_b64_tr_b16 %0, %1 offset:%2" : "=v"(c.vup[db][ks]) : "v"(c.vvb + vslot), "i"(4096 * ks + 2048 + 512 * db) : "memory");
}
template <int I> DI void k_read(W4C& c, unsigned slot) {
  constexpr int kb = I >> 3, d0 = I & 7;
  asm volatile("ds_read_b128 a[%c1:%c2], %0 offset:%c3" :: "v"(c.vkb[d0 & 3] + slot), "i"(AK_(kb, d0)), "i"(AK_(kb, d0) + 3), "i"(8192 * kb + 128 * (d0 >> 2)) : "memory", W4_ACLOB_HI);
}
template <int I> DI void q_write1(const u32x4& w) {
  constexpr int aq = AQ_(I >> 3, I & 7);
  asm volatile("v_accvgpr_write_b32 a[%c4], %0\n\tv_accvgpr_write_b32 a[%c5], %1\n\tv_accvgpr_write_b32 a[%c6], %2\n\tv_accvgpr_write_b32 a[%c7], %3"
               :: "v"(w[0]), "v"(w[1]), "v"(w[2]), "v"(w[3]), "i"(aq), "i"(aq + 1), "i"(aq + 2), "i"(aq + 3) : W4_ACLOB_HI);
}
template <int X, bool FIRST, int M> DI void qk_mfma(W4C& c) {
  constexpr int d0 = M >> 2, kb = (M >> 1) & 1, qb = M & 1, ak = AK_(kb, d0), aq = AQ_(qb, d0);
  if constexpr (d0 == 0) {
    if constexpr (FIRST) asm volatile("v_mfma_f32_32x32x16_bf16 %0, a[%c1:%c2], a[%c3:%c4], 0" : "=&v"(c.S[X][kb][qb]) : "i"(ak), "i"(ak + 3), "i"(aq), "i"(aq + 3) : W4_ACLOB, W4_ACLOB_HI);
    else                 asm volatile("v_mfma_f32_32x32x16_bf16 %0, a[%c2:%c3], a[%c4:%c5], %1" : "=&v"(c.S[X][kb][qb]) : "v"(c.NEGM[qb]), "i"(ak), "i"(ak + 3), "i"(aq), "i"(aq + 3) : W4_ACLOB, W4_ACLOB_HI);
  } else asm volatile("v_mfma_f32_32x32x16_bf16 %0, a[%c1:%c2], a[%c3:%c4], %0" : "+v"(c.S[X][kb][qb]) : "i"(ak), "i"(ak + 3), "i"(aq), "i"(aq + 3) : W4_ACLOB, W4_ACLOB_HI);
}
template <int M, bool ZERO> DI void pv_mfma(W4C& c) {
  constexpr int ks = M >> 3, db = (M >> 1) & 3, qb = M & 1, ao = AO_(db, qb);
  if constexpr (ZERO && ks == 0) asm volatile("v_mfma_f32_32x32x16_bf16 a[%c2:%c3], %0, %1, 0" :: "v"(c.Vf[db][ks]), "v"(c.Pk[qb][ks]), "i"(ao), "i"(ao + 15) : W4_ACLOB);
  else                         asm volatile("v_mfma_f32_32x32x16_bf16 a[%c2:%c3], %0, %1, a[%c2:%c3]" :: "v"(c.Vf[db][ks]), "v"(c.Pk[qb][ks]), "i"(ao), "i"(ao + 15) : W4_ACLOB);
}
template <int X, int QB, int KS, int P> DI void pack1(W4C& c) {
  u32x4 w = __builtin_bit_cast(u32x4, c.Pk[QB][KS]);
  w[P] = cvtpk(c.S[X][KS >> 1][QB][8 * (KS & 1) + 2 * P], c.S[X][KS >> 1][QB][8 * (KS & 1) + 2 * P + 1]);
  c.Pk[QB][KS] = __builtin_bit_cast(bf16x8, w);
}
template <int X, int J> DI void rowsum1(W4C& c) {
  constexpr int qb = J >> 4, ks = (J >> 2) & 3, p = J & 3;
  c.rs0[qb] = fadd_s(c.rs0[qb], c.S[X][ks >> 1][qb][8 * (ks & 1) + 2 * p]); c.rs1[qb] = fadd_s(c.rs1[qb], c.S[X][ks >> 1][qb][8 * (ks & 1) + 2 * p + 1]);
}

template <int R> DI void acc_scale1(float f) {
  float t;
  asm volatile("v_accvgpr_read_b32 %0, a[%c1]\n\tv_mul_f32 %0, %2, %0\n\tv_accvgpr_write_b32 a[%c1], %0" : "=&v"(t) : "i"(R), "v"(f) : W4_ACLOB);
}
template <int... Rs> DI void acc_scale_all(float f0, float f1, iseq<Rs...>) { (acc_scale1<Rs>(((Rs >> 4) & 1) ? f1 : f0), ...); }
template <int QB, int... Rs> DI void acc_scale_qb(float f, iseq<Rs...>) { (acc_scale1<16 * (2 * (Rs >> 4) + QB) + (Rs & 15)>(f), ...); }
template <int R> DI float acc_read1() { float t; asm volatile("v_accvgpr_read_b32 %0, a[%c1]" : "=v"(t) : "i"(R)); return t; }
template <int DB, int QB, int... Rs> DI void acc_read16(float (&o)[16], iseq<Rs...>) { ((o[Rs] = acc_read1<AO_(DB, QB) + Rs>()), ...); }

template <int X, bool FIRST, int M> DI void gapA(W4C& c, unsigned vslot, const int t) {
  constexpr int Xp = X ^ 1;
  qk_mfma<X, FIRST, M>(c);
  if constexpr (M >= 16 && M <= 31) {
    const int tk = t + 2, tv = t + 1;
    constexpr int q = (M - 16) >> 1;
    if constexpr (((M - 16) & 1) == 0) {
      if constexpr (q < 4) c.pm0[q] = c.m0k + (unsigned)((t + 2) & 1) * 0x4000u + 1024u * q; else c.pm0[q] = c.m0v + (unsigned)((t + 1) & 1) * 0x4000u + 1024u * (q - 4);
      asm volatile("" : "+s"(c.pm0[q]));
    } else {
      if constexpr (q < 4) c.psf[q] = KTILEB * (unsigned)tk + c.sofk + KRS * 4u * q; else c.psf[q] = KTILEB * (unsigned)tv + c.sofv + KRS * 8u * ((q - 4) >> 1) + 128u * ((q - 4) & 1);
      asm volatile("" : "+s"(c.psf[q]));
    }
  }
  if constexpr (M == 31) asm volatile("s_nop 4" : "+s"(c.pm0[0]), "+s"(c.pm0[1]), "+s"(c.pm0[2]), "+s"(c.pm0[3]), "+s"(c.pm0[4]), "+s"(c.pm0[5]), "+s"(c.pm0[6]), "+s"(c.pm0[7]),
                                                  "+s"(c.psf[0]), "+s"(c.psf[1]), "+s"(c.psf[2]), "+s"(c.psf[3]), "+s"(c.psf[4]), "+s"(c.psf[5]), "+s"(c.psf[6]), "+s"(c.psf[7]));
  if constexpr (!FIRST && M == 8) asm volatile("" :: "v"(c.NEGM[0]), "v"(c.NEGM[1]));
  if constexpr (!FIRST) {
    constexpr int base = M + (M >= 4) + (M >= 8) + (M >= 12) + (M >= 16) + (M >= 20) + (M >= 24);
    if constexpr (M <= 25) tr_read<base>(c, vslot);
    if constexpr (M == 3 || M == 7 || M == 11 || M == 15 || M == 19 || M == 23) tr_read<base + 1>(c, vslot);
    if constexpr (M < 16) {
      constexpr int e0 = 2 * M, e1 = 2 * M + 1;
      c.S[Xp][1][e0 >> 4][e0 & 15] = __builtin_amdgcn_exp2f(c.S[Xp][1][e0 >> 4][e0 & 15]);
      c.S[Xp][1][e1 >> 4][e1 & 15] = __builtin_amdgcn_exp2f(c.S[Xp][1][e1 >> 4][e1 & 15]);
      pack1<Xp, (M >> 3), ((M >> 2) & 1), (M & 3)>(c);
    } else {
      constexpr int cc = M - 16;
      pack1<Xp, (cc >> 3), 2 + ((cc >> 2) & 1), (cc & 3)>(c);
      rowsum1<Xp, 16 * (cc >> 3) + 8 + 4 * ((cc >> 2) & 1) + (cc & 3)>(c);
    }
  }
  GAP();
}
#define W4_VLO "+v"(c.vlo[0][0]), "+v"(c.vlo[0][1]), "+v"(c.vlo[0][2]), "+v"(c.vlo[0][3]), "+v"(c.vlo[1][0]), "+v"(c.vlo[1][1]), "+v"(c.vlo[1][2]), "+v"(c.vlo[1][3]), \
               "+v"(c.vlo[2][0]), "+v"(c.vlo[2][1]), "+v"(c.vlo[2][2]), "+v"(c.vlo[2][3]), "+v"(c.vlo[3][0]), "+v"(c.vlo[3][1]), "+v"(c.vlo[3][2]), "+v"(c.vlo[3][3])
#define W4_VUP "+v"(c.vup[0][0]), "+v"(c.vup[0][1]), "+v"(c.vup[0][2]), "+v"(c.vup[0][3]), "+v"(c.vup[1][0]), "+v"(c.vup[1][1]), "+v"(c.vup[1][2]), "+v"(c.vup[1][3]), \
               "+v"(c.vup[2][0]), "+v"(c.vup[2][1]), "+v"(c.vup[2][2]), "+v"(c.vup[2][3]), "+v"(c.vup[3][0]), "+v"(c.vup[3][1]), "+v"(c.vup[3][2]), "+v"(c.vup[3][3])
template <int X, bool FIRST, int... Ms> DI void phaseA_(W4C& c, const int t, iseq<Ms...>) {
  const unsigned vslot = (unsigned)((t - 1) & 1) * 0x4000u;
  (gapA<X, FIRST, Ms>(c, vslot, t), ...);
  if constexpr (!FIRST) {
    #pragma unroll
    for (int qb = 0; qb < 2; ++qb) {
      const float rs = fadd_s(c.rs0[qb], c.rs1[qb]);
      auto pr = __builtin_amdgcn_permlane32_swap(__float_as_uint(rs), __float_as_uint(rs), false, false);
      c.lsum[qb] = fadd_s(c.lsum[qb], fadd_s(__uint_as_float(pr[0]), __uint_as_float(pr[1])));
    }
  }
  if constexpr (FIRST) asm volatile("s_waitcnt vmcnt(0) lgkmcnt(0)\n\ts_barrier" ::: "memory");
  else { asm volatile("s_waitcnt vmcnt(0) lgkmcnt(0)" : W4_VLO :: "memory"); asm volatile("s_barrier" : W4_VUP :: "memory"); }
  GAP();
  if constexpr (!FIRST) {
    #pragma unroll
    for (int ks = 0; ks < 4; ++ks)
      #pragma unroll
      for (int db = 0; db < 4; ++db) c.Vf[db][ks] = __builtin_shufflevector(c.vlo[db][ks], c.vup[db][ks], 0, 1, 2, 3, 4, 5, 6, 7);
  }
}
template <int X, bool FIRST> DI void phaseA(W4C& c, const int t) { phaseA_<X, FIRST>(c, t, make_iseq<32>{}); }

template <int X, bool PEEL, bool ZERO1, bool DK, bool DV, bool KR, int M> DI void gapB(W4C& c, const unsigned kslot_rd) {
  if constexpr (!PEEL) pv_mfma<M, ZERO1>(c);
  if constexpr (M < 4 && DK)             asm volatile("s_mov_b32 m0, %0\n\ts_nop 0\n\tbuffer_load_dwordx4 %1, %2, %3 offen lds" :: "s"(c.pm0[M]), "v"(c.vksrc[M & 1]), "s"(c.srdK),  "s"(c.psf[M]) : "m0", "memory");
  else if constexpr (M >= 4 && M < 8 && DV) asm volatile("s_mov_b32 m0, %0\n\ts_nop 0\n\tbuffer_load_dwordx4 %1, %2, %3 offen lds" :: "s"(c.pm0[M]), "v"(c.vvsrc), "s"(c.srdV),  "s"(c.psf[M]) : "m0", "memory");
  if constexpr (M >= 1 && M <= 16 && KR) k_read<M - 1>(c, kslot_rd);
  if constexpr (M >= 8 && M < 16) {
    constexpr int i = M - 8, q = i >> 2, r = 4 * (i & 3);
    if constexpr ((i & 3) == 0) { c.pmx0[q] = max3f(c.S[X][0][q][0], c.S[X][0][q][1], c.S[X][1][q][0]); c.pmx1[q] = max3f(c.S[X][0][q][2], c.S[X][0][q][3], c.S[X][1][q][1]);
                                  c.pmx0[q] = max3f(c.pmx0[q], c.S[X][1][q][2], c.S[X][1][q][3]); }
    else                        { c.pmx0[q] = max3f(c.pmx0[q], c.S[X][0][q][r], c.S[X][0][q][r + 1]); c.pmx1[q] = max3f(c.pmx1[q], c.S[X][0][q][r + 2], c.S[X][0][q][r + 3]);
                                  c.pmx0[q] = max3f(c.pmx0[q], c.S[X][1][q][r], c.S[X][1][q][r + 1]); c.pmx1[q] = max3f(c.pmx1[q], c.S[X][1][q][r + 2], c.S[X][1][q][r + 3]); }
  }
  if constexpr (M == 16 || M == 17) {
    constexpr int q = M - 16; const float mm = max2f(c.pmx0[q], c.pmx1[q]);
    auto pr = __builtin_amdgcn_permlane32_swap(__float_as_uint(mm), __float_as_uint(mm), false, false);
    c.rmax[q] = max2f(__uint_as_float(pr[0]), __uint_as_float(pr[1]));
  }
  if constexpr (M == 18) {
    if constexpr (PEEL) {
      #pragma unroll
      for (int q = 0; q < 2; ++q) { c.mhat[q] = c.rmax[q]; c.lsum[q] = 0.f; c.fct[q] = 1.f;
        #pragma unroll
        for (int kb = 0; kb < 2; ++kb) { _Pragma("unroll") for (int e = 0; e < 16; ++e) c.S[X][kb][q][e] = fsub_s(c.S[X][kb][q][e], c.rmax[q]); }
        negm_bcast(c.NEGM[q], w4_aone(), -c.mhat[q]);
      }
    } else {
      const bool grew = (c.rmax[0] > c.THRv) | (c.rmax[1] > c.THRv);
      if (__builtin_expect(__any((int)grew), 0)) {
        #pragma unroll
        for (int q = 0; q < 2; ++q) { const float dl = max2f(c.rmax[q], 0.f); c.mhat[q] = fadd_s(c.mhat[q], dl); c.fct[q] = __builtin_amdgcn_exp2f(-dl);
          #pragma unroll
          for (int kb = 0; kb < 2; ++kb) { _Pragma("unroll") for (int e = 0; e < 16; ++e) c.S[X][kb][q][e] = fsub_s(c.S[X][kb][q][e], dl); }
          negm_bcast(c.NEGM[q], w4_aone(), -c.mhat[q]);
        }
        c.pend = true;
      }
    }
  }
  if constexpr (M == 18) { c.rs0[0] = c.rs0[1] = c.rs1[0] = c.rs1[1] = 0.f; }
  if constexpr (M >= 19) {
    constexpr int g = M - 19, e_lo = (32 * g) / 13, e_hi = (32 * (g + 1)) / 13;
    {
      constexpr int p_lo = g ? (32 * (g - 1)) / 13 : 0, p_hi = g ? (32 * g) / 13 : 0;
      if constexpr (p_lo + 0 < p_hi) { constexpr int e = p_lo + 0; if constexpr (e & 1) c.rs1[e >> 4] = fadd_s(c.rs1[e >> 4], c.S[X][0][e >> 4][e & 15]); else c.rs0[e >> 4] = fadd_s(c.rs0[e >> 4], c.S[X][0][e >> 4][e & 15]); }
      if constexpr (p_lo + 1 < p_hi) { constexpr int e = p_lo + 1; if constexpr (e & 1) c.rs1[e >> 4] = fadd_s(c.rs1[e >> 4], c.S[X][0][e >> 4][e & 15]); else c.rs0[e >> 4] = fadd_s(c.rs0[e >> 4], c.S[X][0][e >> 4][e & 15]); }
      if constexpr (p_lo + 2 < p_hi) { constexpr int e = p_lo + 2; if constexpr (e & 1) c.rs1[e >> 4] = fadd_s(c.rs1[e >> 4], c.S[X][0][e >> 4][e & 15]); else c.rs0[e >> 4] = fadd_s(c.rs0[e >> 4], c.S[X][0][e >> 4][e & 15]); }
    }
    if constexpr (e_lo + 0 < e_hi) c.S[X][0][(e_lo + 0) >> 4][(e_lo + 0) & 15] = __builtin_amdgcn_exp2f(c.S[X][0][(e_lo + 0) >> 4][(e_lo + 0) & 15]);
    if constexpr (e_lo + 1 < e_hi) c.S[X][0][(e_lo + 1) >> 4][(e_lo + 1) & 15] = __builtin_amdgcn_exp2f(c.S[X][0][(e_lo + 1) >> 4][(e_lo + 1) & 15]);
    if constexpr (e_lo + 2 < e_hi) c.S[X][0][(e_lo + 2) >> 4][(e_lo + 2) & 15] = __builtin_amdgcn_exp2f(c.S[X][0][(e_lo + 2) >> 4][(e_lo + 2) & 15]);
  }
  if constexpr (M == 31) asm volatile("" :: "v"(c.S[X][0][0]), "v"(c.S[X][0][1]), "v"(c.NEGM[0]), "v"(c.NEGM[1]), "v"(c.rs0[0]), "v"(c.rs0[1]), "v"(c.rs1[0]), "v"(c.rs1[1]));
  GAP();
}
template <int X, bool PEEL, bool ZERO1, bool DK, bool DV, bool KR, int... Ms> DI void phaseB_(W4C& c, const int t, iseq<Ms...>) {
  const unsigned kslot_rd = (unsigned)((t + 1) & 1) * 0x4000u;
  if constexpr (!PEEL) asm volatile("s_nop 1" ::: "memory");
  GAP();
  (gapB<X, PEEL, ZERO1, DK, DV, KR, Ms>(c, kslot_rd), ...);
  {
    #pragma unroll
    for (int e = (32 * 12) / 13; e < 32; ++e) { if (e & 1) c.rs1[e >> 4] = fadd_s(c.rs1[e >> 4], c.S[X][0][e >> 4][e & 15]); else c.rs0[e >> 4] = fadd_s(c.rs0[e >> 4], c.S[X][0][e >> 4][e & 15]); }
  }
  if constexpr (!PEEL) {
    if (__builtin_expect(c.pend, 0)) {
      asm volatile("s_nop 7\n\ts_nop 7" ::: "memory");
      acc_scale_all(c.fct[0], c.fct[1], make_iseq<128>{});
      asm volatile("s_nop 1" ::: "memory");
      c.lsum[0] = fmul_s(c.lsum[0], c.fct[0]); c.lsum[1] = fmul_s(c.lsum[1], c.fct[1]); c.pend = false;
    }
  }
  asm volatile("s_waitcnt lgkmcnt(0)" ::: "memory");
  GAP();
}
template <int X, bool PEEL, bool ZERO1, bool DK = true, bool DV = true, bool KR = true> DI void phaseB(W4C& c, const int t) { phaseB_<X, PEEL, ZERO1, DK, DV, KR>(c, t, make_iseq<32>{}); }

template <int... Is> DI void k_reads(W4C& c, unsigned slot, iseq<Is...>) { (k_read<Is>(c, slot), ...); }
template <int... Is> DI void tr_reads(W4C& c, unsigned vslot, iseq<Is...>) { (tr_read<Is>(c, vslot), ...); }
template <int X, int... Cs> DI void packs(W4C& c, iseq<Cs...>) { (pack1<X, ((Cs >> 3) & 1), 2 * (Cs >> 4) + ((Cs >> 2) & 1), (Cs & 3)>(c), ...); }
template <int X, int J> DI void rowsum1_padded(W4C& c) { rowsum1<X, J>(c); asm volatile("s_nop 1"); }
template <int X, int... Js> DI void rowsums(W4C& c, iseq<Js...>) { (rowsum1_padded<X, Js>(c), ...); }
template <int... Ms> DI void pv_drain(W4C& c, iseq<Ms...>) { ((pv_mfma<Ms, false>(c), GAP()), ...); }

template <int QB, int D0> DI void tree_qk(f32x16& S, const bf16x8& kf) {
  constexpr int aq = AQ_(QB, D0);
  if constexpr (D0 == 7) asm volatile("s_nop 1\n\tv_mfma_f32_32x32x16_bf16 %0, %1, a[%c2:%c3], %0\n\ts_nop 11" : "+v"(S) : "v"(kf), "i"(aq), "i"(aq + 3) : W4_ACLOB, W4_ACLOB_HI);
  else                   asm volatile("s_nop 1\n\tv_mfma_f32_32x32x16_bf16 %0, %1, a[%c2:%c3], %0" : "+v"(S) : "v"(kf), "i"(aq), "i"(aq + 3) : W4_ACLOB, W4_ACLOB_HI);
}
template <int QB, int DB> DI void tree_pv(const bf16x8& vf, const bf16x8& pk) {
  constexpr int ao = AO_(DB, QB);
  asm volatile("s_nop 1\n\tv_mfma_f32_32x32x16_bf16 a[%c2:%c3], %0, %1, a[%c2:%c3]" :: "v"(vf), "v"(pk), "i"(ao), "i"(ao + 15) : W4_ACLOB);
}
DI float bf16r(float x) { unsigned u = cvtpk(x, 0.f) & 0xFFFFu; return __uint_as_float(u << 16); }
DI float rec_bias(unsigned rec) {
  const unsigned mb = rec >> 16;
  if (mb == 0x3F80u) return 0.f;
  const float mv = __uint_as_float(mb << 16);
  return bf16r(bf16r(1.0f - mv) * -1.0e9f) * 1.4426950408889634f;
}
template <int QB> DI void tree_qblock(W4C& c, char* lds, const unsigned* __restrict__ cnts, const unsigned* __restrict__ recs, const char* __restrict__ Kt, const char* __restrict__ Vt,
                                      unsigned grp, unsigned wid, unsigned lane) {
  const unsigned l32 = lane & 31u, hi = lane >> 5;
  const unsigned cnt = cnts[grp];
  const unsigned* rg = recs + (size_t)grp * REC_CAP;
  char* vst = lds + TREEV + 8192u * wid;
  unsigned* rst = (unsigned*)(lds + TREEREC + 128u * wid);
  const unsigned vvt = c.ldsb + TREEV + 8192u * wid + (((lane & 3u) << 3) | (((lane >> 2) & 3u) << 6) | (((lane >> 4) & 1u) << 5) | (hi << 8));
  for (unsigned base = 0; base < cnt; base += 32u) {
    unsigned rec = REC_INVALID;
    if (base + l32 < cnt) rec = rg[base + l32];
    const unsigned key = rec & 0x7FFu;
    bf16x8 kf[8];
    #pragma unroll
    for (int d0 = 0; d0 < 8; ++d0) kf[d0] = *(const bf16x8*)(Kt + key * 256u + (2u * d0 + hi) * 16u);
    if (hi == 0) rst[l32] = rec;
    asm volatile("s_waitcnt lgkmcnt(0)" ::: "memory");
    u32x4 vv[8];
    #pragma unroll
    for (int g8 = 0; g8 < 4; ++g8) {
      const unsigned vk = rst[8 * g8 + ((lane >> 2) & 7u)] & 0x7FFu;
      #pragma unroll
      for (int ch = 0; ch < 2; ++ch) vv[2 * g8 + ch] = *(const u32x4*)(Vt + vk * 256u + 128u * ch + 64u * hi + 16u * (lane & 3u));
    }
    f32x16 S;
    #pragma unroll
    for (int g4 = 0; g4 < 4; ++g4) {
      const u32x4 r4 = *(const u32x4*)(rst + 8 * g4 + 4 * hi);
      #pragma unroll
      for (int k = 0; k < 4; ++k) {
        const bool own = ((r4[k] >> 11) & 15u) == (l32 & 7u);
        S[4 * g4 + k] = own ? (rec_bias(r4[k]) - c.mhat[QB]) : -1.0e30f;
      }
    }
    #pragma unroll
    for (int i = 0; i < 8; ++i) *(u32x4*)(vst + 1024u * i + 16u * lane) = vv[i];
    tree_qk<QB, 0>(S, kf[0]); tree_qk<QB, 1>(S, kf[1]); tree_qk<QB, 2>(S, kf[2]); tree_qk<QB, 3>(S, kf[3]);
    tree_qk<QB, 4>(S, kf[4]); tree_qk<QB, 5>(S, kf[5]); tree_qk<QB, 6>(S, kf[6]); tree_qk<QB, 7>(S, kf[7]);
    float mx = max3f(S[0], S[1], S[2]);
    mx = max3f(mx, S[3], S[4]); mx = max3f(mx, S[5], S[6]); mx = max3f(mx, S[7], S[8]); mx = max3f(mx, S[9], S[10]);
    mx = max3f(mx, S[11], S[12]); mx = max3f(mx, S[13], S[14]); mx = max2f(mx, S[15]);
    { auto pr = __builtin_amdgcn_permlane32_swap(__float_as_uint(mx), __float_as_uint(mx), false, false);
      mx = max2f(__uint_as_float(pr[0]), __uint_as_float(pr[1])); }
    if (__builtin_expect(__any((int)(mx > c.THRv)), 0)) {
      const float dl = max2f(mx, 0.f), f = __builtin_amdgcn_exp2f(-dl);
      c.mhat[QB] = fadd_s(c.mhat[QB], dl);
      #pragma unroll
      for (int e = 0; e < 16; ++e) S[e] = fsub_s(S[e], dl);
      asm volatile("s_nop 7\n\ts_nop 7" ::: "memory");
      acc_scale_qb<QB>(f, make_iseq<64>{});
      asm volatile("s_nop 1" ::: "memory");
      c.lsum[QB] = fmul_s(c.lsum[QB], f);
    }
    float rs = 0.f;
    #pragma unroll
    for (int e = 0; e < 16; ++e) { S[e] = __builtin_amdgcn_exp2f(S[e]); rs += S[e]; }
    { auto pr = __builtin_amdgcn_permlane32_swap(__float_as_uint(rs), __float_as_uint(rs), false, false);
      c.lsum[QB] = c.lsum[QB] + __uint_as_float(pr[0]) + __uint_as_float(pr[1]); }
    bf16x8 pk[2];
    #pragma unroll
    for (int ks = 0; ks < 2; ++ks) { u32x4 w;
      #pragma unroll
      for (int p = 0; p < 4; ++p) w[p] = cvtpk(S[8 * ks + 2 * p], S[8 * ks + 2 * p + 1]);
      pk[ks] = __builtin_bit_cast(bf16x8, w); }
    bf16x4 lo[4][2], up[4][2];
    asm volatile("s_waitcnt lgkmcnt(0)" ::: "memory");
    #pragma unroll
    for (int ks = 0; ks < 2; ++ks)
      #pragma unroll
      for (int db = 0; db < 4; ++db) {
        asm volatile("ds_read_b64_tr_b16 %0, %1 offset:%2" : "=v"(lo[db][ks]) : "v"(vvt), "i"(4096 * ks + 512 * db) : "memory");
        asm volatile("ds_read_b64_tr_b16 %0, %1 offset:%2" : "=v"(up[db][ks]) : "v"(vvt), "i"(4096 * ks + 2048 + 512 * db) : "memory");
      }
    asm volatile("s_waitcnt lgkmcnt(0)" : "+v"(lo[0][0]), "+v"(lo[1][0]), "+v"(lo[2][0]), "+v"(lo[3][0]), "+v"(lo[0][1]), "+v"(lo[1][1]), "+v"(lo[2][1]), "+v"(lo[3][1]),
                                          "+v"(up[0][0]), "+v"(up[1][0]), "+v"(up[2][0]), "+v"(up[3][0]), "+v"(up[0][1]), "+v"(up[1][1]), "+v"(up[2][1]), "+v"(up[3][1]) :: "memory");
    #pragma unroll
    for (int ks = 0; ks < 2; ++ks) {
      tree_pv<QB, 0>(__builtin_shufflevector(lo[0][ks], up[0][ks], 0, 1, 2, 3, 4, 5, 6, 7), pk[ks]);
      tree_pv<QB, 1>(__builtin_shufflevector(lo[1][ks], up[1][ks], 0, 1, 2, 3, 4, 5, 6, 7), pk[ks]);
      tree_pv<QB, 2>(__builtin_shufflevector(lo[2][ks], up[2][ks], 0, 1, 2, 3, 4, 5, 6, 7), pk[ks]);
      tree_pv<QB, 3>(__builtin_shufflevector(lo[3][ks], up[3][ks], 0, 1, 2, 3, 4, 5, 6, 7), pk[ks]);
    }
  }
  asm volatile("s_nop 7\n\ts_nop 7" ::: "memory");
}

extern "C" __global__ __launch_bounds__(256, 1)
void tree_attn_main(const unsigned short* __restrict__ Q, const unsigned short* __restrict__ Kc, const unsigned short* __restrict__ Vc,
                    const unsigned short* __restrict__ Ktr, const unsigned short* __restrict__ Vtr,
                    const unsigned* __restrict__ cnts, const unsigned* __restrict__ recs, unsigned short* __restrict__ O) {
  __shared__ __attribute__((aligned(16))) char lds[LDS_BYTES];
  const unsigned tid = threadIdx.x, lane = tid & 63u, wid = tid >> 6, l32 = lane & 31u, hi = lane >> 5;
  const float CSC = 0.12751743082f;
  W4C c;
  c.THRv = THR;
  c.ldsb = (unsigned)(size_t)lds;
  #pragma unroll
  for (int j = 0; j < 4; ++j) c.vkb[j] = c.ldsb + KRING + 256u * l32 + 16u * ((2u * j + hi) ^ (l32 & 7u));
  c.vvb = c.ldsb + VRING + (((lane & 3u) << 3) | (((lane >> 2) & 3u) << 6) | (((lane >> 4) & 1u) << 5) | (hi << 8));
  #pragma unroll
  for (int par = 0; par < 2; ++par) c.vksrc[par] = KRS * (lane >> 4) + 16u * ((lane & 15u) ^ (4u * par + (lane >> 4)));
  c.vvsrc = KRS * ((lane >> 2) & 7u) + 64u * hi + 16u * (lane & 3u);
  c.m0k = rfl(c.ldsb + KRING + 4096u * wid); c.m0v = rfl(c.ldsb + VRING + 4096u * wid);
  c.sofk = rfl(KRS * 16u * wid); c.sofv = rfl(KRS * 16u * wid);

  const unsigned blk = blockIdx.x, kvh = blk & 7u, pblk = blk >> 3;
  const unsigned P0 = 64u * pblk + 16u * wid;
  const char* Kbase = (const char*)Kc + (size_t)kvh * C * 256;
  const char* Vbase = (const char*)Vc + (size_t)kvh * C * 256;
  c.srdK = __builtin_amdgcn_make_buffer_rsrc((void*)Kbase, (short)0, C * 256, SRD_WORD3);
  c.srdV = __builtin_amdgcn_make_buffer_rsrc((void*)Vbase, (short)0, C * 256, SRD_WORD3);
  auto dma_k = [&](int tk) {
    const unsigned slot = (unsigned)(tk & 1) * 0x4000u, skv = KTILEB * (unsigned)tk;
    #pragma unroll
    for (int q = 0; q < 4; ++q)
      asm volatile("s_nop 4\n\ts_mov_b32 m0, %0\n\ts_nop 0\n\tbuffer_load_dwordx4 %1, %2, %3 offen lds"
                   :: "s"(rfl(c.m0k + slot + 1024u * q)), "v"(c.vksrc[q & 1]), "s"(c.srdK), "s"(rfl(skv + c.sofk + KRS * 4u * q)) : "m0", "memory");
  };
  auto dma_v = [&](int tv) {
    const unsigned slot = (unsigned)(tv & 1) * 0x4000u, skv = KTILEB * (unsigned)tv;
    #pragma unroll
    for (int q = 0; q < 4; ++q)
      asm volatile("s_nop 4\n\ts_mov_b32 m0, %0\n\ts_nop 0\n\tbuffer_load_dwordx4 %1, %2, %3 offen lds"
                   :: "s"(rfl(c.m0v + slot + 1024u * q)), "v"(c.vvsrc), "s"(c.srdV), "s"(rfl(skv + c.sofv + KRS * 8u * (q >> 1) + 128u * (q & 1))) : "m0", "memory");
  };
  bf16x8 qf[2][8];
  #pragma unroll
  for (int qb = 0; qb < 2; ++qb) {
    const char* qrow = (const char*)Q + ((size_t)(4u * kvh + (l32 >> 3)) * T + P0 + 8u * qb + (l32 & 7u)) * 256u + hi * 16u;
    #pragma unroll
    for (int d0 = 0; d0 < 8; ++d0) qf[qb][d0] = *(const bf16x8*)(qrow + 32 * d0);
  }
  dma_k(0); dma_v(0); dma_k(1);
  {
    u32x4 w;
    #define QW(I) { const bf16x8 q8 = qf[(I) >> 3][(I) & 7]; _Pragma("unroll") for (int e = 0; e < 4; ++e) w[e] = cvtpk(fmul_s((float)q8[2 * e], CSC), fmul_s((float)q8[2 * e + 1], CSC)); q_write1<I>(w); }
    QW(0) QW(1) QW(2) QW(3) QW(4) QW(5) QW(6) QW(7) QW(8) QW(9) QW(10) QW(11) QW(12) QW(13) QW(14) QW(15)
    #undef QW
  }
  asm volatile("s_waitcnt vmcnt(0)\n\ts_barrier" ::: "memory");
  k_reads(c, 0u, make_iseq<16>{});
  asm volatile("s_waitcnt lgkmcnt(0)\n\ts_nop 1" ::: "memory");

  c.mhat[0] = c.mhat[1] = 0.f; c.lsum[0] = c.lsum[1] = 0.f; c.fct[0] = c.fct[1] = 1.f; c.pend = false;

  phaseA<0, true>(c, 0);  phaseB<0, true,  false>(c, 0);
  phaseA<1, false>(c, 1); phaseB<1, false, true>(c, 1);
  for (int t = 2; t < NT - 2; t += 2) {
    phaseA<0, false>(c, t);     phaseB<0, false, false>(c, t);
    phaseA<1, false>(c, t + 1); phaseB<1, false, false>(c, t + 1);
  }
  phaseA<0, false>(c, NT - 2); phaseB<0, false, false,  false,  true,  true>(c, NT - 2);
  phaseA<1, false>(c, NT - 1); phaseB<1, false, false,  false,  false,  false>(c, NT - 1);
  {
    const unsigned vslot = (unsigned)((NT - 1) & 1) * 0x4000u;
    tr_reads(c, vslot, make_iseq<32>{});
    #pragma unroll
    for (int e = 0; e < 32; ++e) c.S[1][1][e >> 4][e & 15] = __builtin_amdgcn_exp2f(c.S[1][1][e >> 4][e & 15]);
    packs<1>(c, make_iseq<32>{});
    rowsums<1>(c, iseq<8, 9, 10, 11, 12, 13, 14, 15, 24, 25, 26, 27, 28, 29, 30, 31>{});
    #pragma unroll
    for (int qb = 0; qb < 2; ++qb) { const float rs = fadd_s(c.rs0[qb], c.rs1[qb]);
      auto pr = __builtin_amdgcn_permlane32_swap(__float_as_uint(rs), __float_as_uint(rs), false, false);
      c.lsum[qb] = fadd_s(c.lsum[qb], fadd_s(__uint_as_float(pr[0]), __uint_as_float(pr[1]))); }
    asm volatile("s_waitcnt lgkmcnt(0)" : W4_VLO :: "memory"); asm volatile("" : W4_VUP :: "memory");
    GAP();
    #pragma unroll
    for (int ks = 0; ks < 4; ++ks)
      #pragma unroll
      for (int db = 0; db < 4; ++db) c.Vf[db][ks] = __builtin_shufflevector(c.vlo[db][ks], c.vup[db][ks], 0, 1, 2, 3, 4, 5, 6, 7);
    asm volatile("s_nop 1" ::: "memory");
    pv_drain(c, make_iseq<32>{});
    asm volatile("s_nop 7\n\ts_nop 7" ::: "memory");
  }
  {
    const char* Kt = (const char*)Ktr + (size_t)kvh * T * 256;
    const char* Vt = (const char*)Vtr + (size_t)kvh * T * 256;
    const unsigned g0 = (P0 >> 3);
    tree_qblock<0>(c, lds, cnts, recs, Kt, Vt, g0, wid, lane);
    tree_qblock<1>(c, lds, cnts, recs, Kt, Vt, g0 + 1u, wid, lane);
  }
  asm volatile("s_barrier" ::: "memory");
  {
    char* stg = lds + 16384u * wid;
    #pragma unroll
    for (int qb = 0; qb < 2; ++qb) {
      const float il = c.lsum[qb] > 0.f ? __builtin_amdgcn_rcpf(c.lsum[qb]) : 0.f;
      #define EPI_DB(DB) { float o[16]; if (qb == 0) acc_read16<DB, 0>(o, make_iseq<16>{}); else acc_read16<DB, 1>(o, make_iseq<16>{}); \
        _Pragma("unroll") for (int g = 0; g < 4; ++g) { u32x2 w; w[0] = cvtpk(o[4 * g] * il, o[4 * g + 1] * il); w[1] = cvtpk(o[4 * g + 2] * il, o[4 * g + 3] * il); \
          *(u32x2*)(stg + 32u * 256u * qb + l32 * 256u + 16u * ((4u * DB + g) ^ (l32 & 15u)) + 8u * hi) = w; } }
      EPI_DB(0) EPI_DB(1) EPI_DB(2) EPI_DB(3)
      #undef EPI_DB
    }
    asm volatile("s_waitcnt lgkmcnt(0)" ::: "memory");
    #pragma unroll
    for (int j = 0; j < 16; ++j) {
      const unsigned r = 4u * j + (lane >> 4), cc = lane & 15u;
      const u32x4 v = *(const u32x4*)(stg + r * 256u + 16u * (cc ^ (r & 15u)));
      char* orow = (char*)O + ((size_t)(4u * kvh + ((r >> 3) & 3u)) * T + P0 + 8u * (r >> 5) + (r & 7u)) * 256u + 16u * cc;
      *(u32x4*)orow = v;
    }
  }
}

__global__ __launch_bounds__(256) void tree_prepass(const u32x4* __restrict__ mask, unsigned* __restrict__ cnts, unsigned* __restrict__ recs) {
  __shared__ unsigned wcnt[4];
  const unsigned g = blockIdx.x, tid = threadIdx.x, lane = tid & 63u, wid = tid >> 6;
  u32x4 v[8];
  #pragma unroll
  for (int r = 0; r < 8; ++r) v[r] = mask[(size_t)(8u * g + r) * 256u + tid];
  unsigned long long bal[8][8];
  unsigned tot = 0;
  #pragma unroll
  for (int r = 0; r < 8; ++r)
    #pragma unroll
    for (int e = 0; e < 8; ++e) {
      const unsigned bits = (v[r][e >> 1] >> (16 * (e & 1))) & 0xFFFFu;
      bal[r][e] = __ballot((bits & 0x7FFFu) != 0u);
      tot += __popcll(bal[r][e]);
    }
  if (lane == 0) wcnt[wid] = tot;
  __syncthreads();
  unsigned base = 0;
  for (unsigned w = 0; w < wid; ++w) base += wcnt[w];
  if (tid == 0) cnts[g] = wcnt[0] + wcnt[1] + wcnt[2] + wcnt[3];
  unsigned* rg = recs + (size_t)g * REC_CAP;
  #pragma unroll
  for (int r = 0; r < 8; ++r)
    #pragma unroll
    for (int e = 0; e < 8; ++e) {
      const unsigned long long m = bal[r][e];
      if (m) {
        const unsigned bits = (v[r][e >> 1] >> (16 * (e & 1))) & 0xFFFFu;
        if ((m >> lane) & 1ull) {
          const unsigned idx = base + __popcll(m & ((1ull << lane) - 1ull));
          rg[idx] = (8u * tid + e) | ((unsigned)r << 11) | (bits << 16);
        }
        base += __popcll(m);
      }
    }
}

extern "C" void kernel_launch(void* const* d_in, const int* in_sizes, int n_in, void* d_out, int out_size, void* d_ws, size_t ws_size, hipStream_t stream) {
  unsigned* cnts = (unsigned*)d_ws;
  unsigned* recs = (unsigned*)((char*)d_ws + 4096);
  hipLaunchKernelGGL(tree_prepass, dim3(256), dim3(256), 0, stream, (const u32x4*)d_in[5], cnts, recs);
  hipLaunchKernelGGL(tree_attn_main, dim3(256), dim3(256), 0, stream, (const unsigned short*)d_in[0], (const unsigned short*)d_in[1], (const unsigned short*)d_in[2],
                     (const unsigned short*)d_in[3], (const unsigned short*)d_in[4], (const unsigned*)cnts, (const unsigned*)recs, (unsigned short*)d_out);
}
```

```cpp
#include <hip/hip_runtime.h>
#include <hip/hip_bf16.h>
#include <stdint.h>
#include <utility>

constexpr int D = 128, HQ = 32, HKV = 8, T = 2048, C = 2048, NT = C / 64;
constexpr float THR = 8.0f;
constexpr unsigned KRS = 256u, KTILEB = 64u * KRS;
constexpr unsigned REC_CAP = 8u * 2048u;
constexpr unsigned REC_INVALID = 0x7800u;

typedef float    f32x16 __attribute__((ext_vector_type(16)));
typedef float    f32x2  __attribute__((ext_vector_type(2)));
typedef float    f32x4  __attribute__((ext_vector_type(4)));
typedef __bf16   bf16x8 __attribute__((ext_vector_type(8)));
typedef __bf16   bf16x4 __attribute__((ext_vector_type(4)));
typedef __bf16   bf16x2 __attribute__((ext_vector_type(2)));
typedef unsigned u32x4  __attribute__((ext_vector_type(4)));
typedef unsigned u32x2  __attribute__((ext_vector_type(2)));
template <int... Is> using iseq = std::integer_sequence<int, Is...>;
template <int N> using make_iseq = std::make_integer_sequence<int, N>;

#define KRING 0x0000u
#define VRING 0x8000u
#define TREEV 0x10000u
#define LDS_BYTES 0x28000u
#define SRD_WORD3 0x00020000
#define DI __device__ __forceinline__
#define GAP() __builtin_amdgcn_sched_barrier(0)
#define W4_ACLOB_HI "a128","a129","a130","a131","a132","a133","a134","a135","a136","a137","a138","a139","a140","a141","a142","a143","a144","a145","a146","a147","a148","a149","a150","a151","a152","a153","a154","a155","a156","a157","a158","a159","a160","a161","a162","a163","a164","a165","a166","a167","a168","a169","a170","a171","a172","a173","a174","a175","a176","a177","a178","a179","a180","a181","a182","a183","a184","a185","a186","a187","a188","a189","a190","a191","a192","a193","a194","a195","a196","a197","a198","a199","a200","a201","a202","a203","a204","a205","a206","a207","a208","a209","a210","a211","a212","a213","a214","a215","a216","a217","a218","a219","a220","a221","a222","a223","a224","a225","a226","a227","a228","a229","a230","a231","a232","a233","a234","a235","a236","a237","a238","a239","a240","a241","a242","a243","a244","a245","a246","a247","a248","a249","a250","a251","a252","a253","a254","a255"
#define W4_ACLOB "a0","a1","a2","a3","a4","a5","a6","a7","a8","a9","a10","a11","a12","a13","a14","a15","a16","a17","a18","a19","a20","a21","a22","a23","a24","a25","a26","a27","a28","a29","a30","a31","a32","a33","a34","a35","a36","a37","a38","a39","a40","a41","a42","a43","a44","a45","a46","a47","a48","a49","a50","a51","a52","a53","a54","a55","a56","a57","a58","a59","a60","a61","a62","a63","a64","a65","a66","a67","a68","a69","a70","a71","a72","a73","a74","a75","a76","a77","a78","a79","a80","a81","a82","a83","a84","a85","a86","a87","a88","a89","a90","a91","a92","a93","a94","a95","a96","a97","a98","a99","a100","a101","a102","a103","a104","a105","a106","a107","a108","a109","a110","a111","a112","a113","a114","a115","a116","a117","a118","a119","a120","a121","a122","a123","a124","a125","a126","a127"

DI unsigned rfl(unsigned x) { return __builtin_amdgcn_readfirstlane(x); }
DI unsigned cvtpk(float lo, float hi) {
  f32x2 v = {lo, hi}; bf16x2 b = __builtin_convertvector(v, bf16x2); return __builtin_bit_cast(unsigned, b);
}
DI float max3f(float a, float b, float c) { float r; asm("v_max3_f32 %0, %1, %2, %3" : "=v"(r) : "v"(a), "v"(b), "v"(c)); return r; }
DI float max2f(float a, float b) { float r; asm("v_max_f32_e32 %0, %1, %2" : "=v"(r) : "v"(a), "v"(b)); return r; }
DI float fadd_s(float a, float b) { float r; asm("v_add_f32_e32 %0, %1, %2" : "=v"(r) : "v"(a), "v"(b)); return r; }
DI float fmul_s(float a, float b) { float r; asm("v_mul_f32_e32 %0, %1, %2" : "=v"(r) : "v"(a), "v"(b)); return r; }
DI float fsub_s(float a, float b) { float r; asm("v_sub_f32_e32 %0, %1, %2" : "=v"(r) : "v"(a), "v"(b)); return r; }
DI float w4_aone() { unsigned l; asm volatile("v_mbcnt_lo_u32_b32 %0, -1, 0\n\tv_mbcnt_hi_u32_b32 %0, -1, %0" : "=v"(l)); return l < 32u ? 1.f : 0.f; }
DI void negm_bcast(f32x16& negm, float aone, float neg_mhat) {
  asm volatile("s_nop 1\n\tv_mfma_f32_32x32x2_f32 %0, %1, %2, 0" : "+v"(negm) : "v"(aone), "v"(neg_mhat));
}

struct W4C {
  unsigned vkb[4], vvb, vksrc[2], vvsrc, m0k, m0v, sofk, sofv, ldsb;
  __amdgpu_buffer_rsrc_t srdK, srdV;
  float THRv;
  f32x16 S[2][2][2];
  f32x16 NEGM[2];
  bf16x8 Vf[4][4];
  bf16x8 Pk[2][4];
  bf16x4 vlo[4][4], vup[4][4];
  float mhat[2], lsum[2], fct[2]; bool pend;
  float rs0[2], rs1[2], pmx0[2], pmx1[2], rmax[2];
  unsigned pm0[8], psf[8];
};

constexpr int AO_(int db, int qb) { return 16 * (2 * db + qb); }
constexpr int AQ_(int qb, int d0) { return 128 + 32 * qb + 4 * d0; }
constexpr int AK_(int kb, int d0) { return 192 + 32 * kb + 4 * d0; }

template <int I> DI void tr_read(W4C& c, unsigned vslot) {
  constexpr int ks = I >> 3, db = (I >> 1) & 3;
  if constexpr ((I & 1) == 0) asm volatile("ds_read_b64_tr_b16 %0, %1 offset:%2" : "=v"(c.vlo[db][ks]) : "v"(c.vvb + vslot), "i"(4096 * ks + 512 * db) : "memory");
  else                        asm volatile("ds_read_b64_tr_b16 %0, %1 offset:%2" : "=v"(c.vup[db][ks]) : "v"(c.vvb + vslot), "i"(4096 * ks + 2048 + 512 * db) : "memory");
}
template <int I> DI void k_read(W4C& c, unsigned slot) {
  constexpr int kb = I >> 3, d0 = I & 7;
  asm volatile("ds_read_b128 a[%c1:%c2], %0 offset:%c3" :: "v"(c.vkb[d0 & 3] + slot), "i"(AK_(kb, d0)), "i"(AK_(kb, d0) + 3), "i"(8192 * kb + 128 * (d0 >> 2)) : "memory", W4_ACLOB_HI);
}
template <int I> DI void q_write1(const u32x4& w) {
  constexpr int aq = AQ_(I >> 3, I & 7);
  asm volatile("v_accvgpr_write_b32 a[%c4], %0\n\tv_accvgpr_write_b32 a[%c5], %1\n\tv_accvgpr_write_b32 a[%c6], %2\n\tv_accvgpr_write_b32 a[%c7], %3"
               :: "v"(w[0]), "v"(w[1]), "v"(w[2]), "v"(w[3]), "i"(aq), "i"(aq + 1), "i"(aq + 2), "i"(aq + 3) : W4_ACLOB_HI);
}
template <int X, bool FIRST, int M> DI void qk_mfma(W4C& c) {
  constexpr int d0 = M >> 2, kb = (M >> 1) & 1, qb = M & 1, ak = AK_(kb, d0), aq = AQ_(qb, d0);
  if constexpr (d0 == 0) {
    asm volatile("v_mfma_f32_32x32x16_bf16 %0, a[%c2:%c3], a[%c4:%c5], %1" : "=&v"(c.S[X][kb][qb]) : "v"(c.NEGM[qb]), "i"(ak), "i"(ak + 3), "i"(aq), "i"(aq + 3) : W4_ACLOB, W4_ACLOB_HI);
  } else asm volatile("v_mfma_f32_32x32x16_bf16 %0, a[%c1:%c2], a[%c3:%c4], %0" : "+v"(c.S[X][kb][qb]) : "i"(ak), "i"(ak + 3), "i"(aq), "i"(aq + 3) : W4_ACLOB, W4_ACLOB_HI);
}
template <int M, bool ZERO> DI void pv_mfma(W4C& c) {
  constexpr int ks = M >> 3, db = (M >> 1) & 3, qb = M & 1, ao = AO_(db, qb);
  if constexpr (ZERO && ks == 0) asm volatile("v_mfma_f32_32x32x16_bf16 a[%c2:%c3], %0, %1, 0" :: "v"(c.Vf[db][ks]), "v"(c.Pk[qb][ks]), "i"(ao), "i"(ao + 15) : W4_ACLOB);
  else                         asm volatile("v_mfma_f32_32x32x16_bf16 a[%c2:%c3], %0, %1, a[%c2:%c3]" :: "v"(c.Vf[db][ks]), "v"(c.Pk[qb][ks]), "i"(ao), "i"(ao + 15) : W4_ACLOB);
}
template <int X, int QB, int KS, int P> DI void pack1(W4C& c) {
  u32x4 w = __builtin_bit_cast(u32x4, c.Pk[QB][KS]);
  w[P] = cvtpk(c.S[X][KS >> 1][QB][8 * (KS & 1) + 2 * P], c.S[X][KS >> 1][QB][8 * (KS & 1) + 2 * P + 1]);
  c.Pk[QB][KS] = __builtin_bit_cast(bf16x8, w);
}
template <int X, int J> DI void rowsum1(W4C& c) {
  constexpr int qb = J >> 4, ks = (J >> 2) & 3, p = J & 3;
  c.rs0[qb] = fadd_s(c.rs0[qb], c.S[X][ks >> 1][qb][8 * (ks & 1) + 2 * p]); c.rs1[qb] = fadd_s(c.rs1[qb], c.S[X][ks >> 1][qb][8 * (ks & 1) + 2 * p + 1]);
}

template <int R> DI void acc_scale1(float f) {
  float t;
  asm volatile("v_accvgpr_read_b32 %0, a[%c1]\n\tv_mul_f32 %0, %2, %0\n\tv_accvgpr_write_b32 a[%c1], %0" : "=&v"(t) : "i"(R), "v"(f) : W4_ACLOB);
}
template <int... Rs> DI void acc_scale_all(float f0, float f1, iseq<Rs...>) { (acc_scale1<Rs>(((Rs >> 4) & 1) ? f1 : f0), ...); }
template <int QB, int... Rs> DI void acc_scale_qb(float f, iseq<Rs...>) { (acc_scale1<16 * (2 * (Rs >> 4) + QB) + (Rs & 15)>(f), ...); }
template <int R> DI float acc_read1() { float t; asm volatile("v_accvgpr_read_b32 %0, a[%c1]" : "=v"(t) : "i"(R)); return t; }
template <int DB, int QB, int... Rs> DI void acc_read16(float (&o)[16], iseq<Rs...>) { ((o[Rs] = acc_read1<AO_(DB, QB) + Rs>()), ...); }

template <int X, bool FIRST, int M> DI void gapA(W4C& c, unsigned vslot, const int t) {
  constexpr int Xp = X ^ 1;
  qk_mfma<X, FIRST, M>(c);
  if constexpr (M >= 16 && M <= 31) {
    const int tk = t + 2, tv = t + 1;
    constexpr int q = (M - 16) >> 1;
    if constexpr (((M - 16) & 1) == 0) {
      if constexpr (q < 4) c.pm0[q] = c.m0k + (unsigned)((t + 2) & 1) * 0x4000u + 1024u * q; else c.pm0[q] = c.m0v + (unsigned)((t + 1) & 1) * 0x4000u + 1024u * (q - 4);
      asm volatile("" : "+s"(c.pm0[q]));
    } else {
      if constexpr (q < 4) c.psf[q] = KTILEB * (unsigned)tk + c.sofk + KRS * 4u * q; else c.psf[q] = KTILEB * (unsigned)tv + c.sofv + KRS * 8u * ((q - 4) >> 1) + 128u * ((q - 4) & 1);
      asm volatile("" : "+s"(c.psf[q]));
    }
  }
  if constexpr (M == 31) asm volatile("s_nop 4" : "+s"(c.pm0[0]), "+s"(c.pm0[1]), "+s"(c.pm0[2]), "+s"(c.pm0[3]), "+s"(c.pm0[4]), "+s"(c.pm0[5]), "+s"(c.pm0[6]), "+s"(c.pm0[7]),
                                                  "+s"(c.psf[0]), "+s"(c.psf[1]), "+s"(c.psf[2]), "+s"(c.psf[3]), "+s"(c.psf[4]), "+s"(c.psf[5]), "+s"(c.psf[6]), "+s"(c.psf[7]));
  if constexpr (M == 8) asm volatile("" :: "v"(c.NEGM[0]), "v"(c.NEGM[1]));
  if constexpr (!FIRST) {
    constexpr int base = M + (M >= 4) + (M >= 8) + (M >= 12) + (M >= 16) + (M >= 20) + (M >= 24);
    if constexpr (M <= 25) tr_read<base>(c, vslot);
    if constexpr (M == 3 || M == 7 || M == 11 || M == 15 || M == 19 || M == 23) tr_read<base + 1>(c, vslot);
    if constexpr (M < 16) {
      constexpr int e0 = 2 * M, e1 = 2 * M + 1;
      c.S[Xp][1][e0 >> 4][e0 & 15] = __builtin_amdgcn_exp2f(c.S[Xp][1][e0 >> 4][e0 & 15]);
      c.S[Xp][1][e1 >> 4][e1 & 15] = __builtin_amdgcn_exp2f(c.S[Xp][1][e1 >> 4][e1 & 15]);
      pack1<Xp, (M >> 3), ((M >> 2) & 1), (M & 3)>(c);
    } else {
      constexpr int cc = M - 16;
      pack1<Xp, (cc >> 3), 2 + ((cc >> 2) & 1), (cc & 3)>(c);
      rowsum1<Xp, 16 * (cc >> 3) + 8 + 4 * ((cc >> 2) & 1) + (cc & 3)>(c);
    }
  }
  GAP();
}
#define W4_VLO "+v"(c.vlo[0][0]), "+v"(c.vlo[0][1]), "+v"(c.vlo[0][2]), "+v"(c.vlo[0][3]), "+v"(c.vlo[1][0]), "+v"(c.vlo[1][1]), "+v"(c.vlo[1][2]), "+v"(c.vlo[1][3]), \
               "+v"(c.vlo[2][0]), "+v"(c.vlo[2][1]), "+v"(c.vlo[2][2]), "+v"(c.vlo[2][3]), "+v"(c.vlo[3][0]), "+v"(c.vlo[3][1]), "+v"(c.vlo[3][2]), "+v"(c.vlo[3][3])
#define W4_VUP "+v"(c.vup[0][0]), "+v"(c.vup[0][1]), "+v"(c.vup[0][2]), "+v"(c.vup[0][3]), "+v"(c.vup[1][0]), "+v"(c.vup[1][1]), "+v"(c.vup[1][2]), "+v"(c.vup[1][3]), \
               "+v"(c.vup[2][0]), "+v"(c.vup[2][1]), "+v"(c.vup[2][2]), "+v"(c.vup[2][3]), "+v"(c.vup[3][0]), "+v"(c.vup[3][1]), "+v"(c.vup[3][2]), "+v"(c.vup[3][3])
template <int X, bool FIRST, int... Ms> DI void phaseA_(W4C& c, const int t, iseq<Ms...>) {
  const unsigned vslot = (unsigned)((t - 1) & 1) * 0x4000u;
  (gapA<X, FIRST, Ms>(c, vslot, t), ...);
  if constexpr (!FIRST) {
    #pragma unroll
    for (int qb = 0; qb < 2; ++qb) {
      const float rs = fadd_s(c.rs0[qb], c.rs1[qb]);
      auto pr = __builtin_amdgcn_permlane32_swap(__float_as_uint(rs), __float_as_uint(rs), false, false);
      c.lsum[qb] = fadd_s(c.lsum[qb], fadd_s(__uint_as_float(pr[0]), __uint_as_float(pr[1])));
    }
  }
  if constexpr (FIRST) asm volatile("s_waitcnt vmcnt(0) lgkmcnt(0)\n\ts_barrier" ::: "memory");
  else { asm volatile("s_waitcnt vmcnt(0) lgkmcnt(0)" : W4_VLO :: "memory"); asm volatile("s_barrier" : W4_VUP :: "memory"); }
  GAP();
  if constexpr (!FIRST) {
    #pragma unroll
    for (int ks = 0; ks < 4; ++ks)
      #pragma unroll
      for (int db = 0; db < 4; ++db) c.Vf[db][ks] = __builtin_shufflevector(c.vlo[db][ks], c.vup[db][ks], 0, 1, 2, 3, 4, 5, 6, 7);
  }
}
template <int X, bool FIRST> DI void phaseA(W4C& c, const int t) { phaseA_<X, FIRST>(c, t, make_iseq<32>{}); }

template <int X, bool PEEL, bool ZERO1, bool DK, bool DV, bool KR, int M> DI void gapB(W4C& c, const unsigned kslot_rd) {
  if constexpr (!PEEL) pv_mfma<M, ZERO1>(c);
  if constexpr (M < 4 && DK)             asm volatile("s_mov_b32 m0, %0\n\ts_nop 0\n\tbuffer_load_dwordx4 %1, %2, %3 offen lds" :: "s"(c.pm0[M]), "v"(c.vksrc[M & 1]), "s"(c.srdK),  "s"(c.psf[M]) : "m0", "memory");
  else if constexpr (M >= 4 && M < 8 && DV) asm volatile("s_mov_b32 m0, %0\n\ts_nop 0\n\tbuffer_load_dwordx4 %1, %2, %3 offen lds" :: "s"(c.pm0[M]), "v"(c.vvsrc), "s"(c.srdV),  "s"(c.psf[M]) : "m0", "memory");
  if constexpr (M >= 1 && M <= 16 && KR) k_read<M - 1>(c, kslot_rd);
  if constexpr (M >= 8 && M < 16) {
    constexpr int i = M - 8, q = i >> 2, r = 4 * (i & 3);
    if constexpr ((i & 3) == 0) { c.pmx0[q] = max3f(c.S[X][0][q][0], c.S[X][0][q][1], c.S[X][1][q][0]); c.pmx1[q] = max3f(c.S[X][0][q][2], c.S[X][0][q][3], c.S[X][1][q][1]);
                                  c.pmx0[q] = max3f(c.pmx0[q], c.S[X][1][q][2], c.S[X][1][q][3]); }
    else                        { c.pmx0[q] = max3f(c.pmx0[q], c.S[X][0][q][r], c.S[X][0][q][r + 1]); c.pmx1[q] = max3f(c.pmx1[q], c.S[X][0][q][r + 2], c.S[X][0][q][r + 3]);
                                  c.pmx0[q] = max3f(c.pmx0[q], c.S[X][1][q][r], c.S[X][1][q][r + 1]); c.pmx1[q] = max3f(c.pmx1[q], c.S[X][1][q][r + 2], c.S[X][1][q][r + 3]); }
  }
  if constexpr (M == 16 || M == 17) {
    constexpr int q = M - 16; const float mm = max2f(c.pmx0[q], c.pmx1[q]);
    auto pr = __builtin_amdgcn_permlane32_swap(__float_as_uint(mm), __float_as_uint(mm), false, false);
    c.rmax[q] = max2f(__uint_as_float(pr[0]), __uint_as_float(pr[1]));
  }
  if constexpr (M == 18) {
    if constexpr (PEEL) {
      const bool grew = (c.rmax[0] > c.THRv) | (c.rmax[1] > c.THRv) | (c.lsum[0] == 0.f) | (c.lsum[1] == 0.f);
      if (__any((int)grew)) {
        #pragma unroll
        for (int q = 0; q < 2; ++q) { const bool emp = c.lsum[q] == 0.f; const float dl = emp ? c.rmax[q] : max2f(c.rmax[q], 0.f); c.mhat[q] = fadd_s(c.mhat[q], dl); c.fct[q] = emp ? 1.f : __builtin_amdgcn_exp2f(-dl);
          #pragma unroll
          for (int kb = 0; kb < 2; ++kb) { _Pragma("unroll") for (int e = 0; e < 16; ++e) c.S[X][kb][q][e] = fsub_s(c.S[X][kb][q][e], dl); }
          negm_bcast(c.NEGM[q], w4_aone(), -c.mhat[q]);
        }
        c.pend = true;
      }
    } else {
      const bool grew = (c.rmax[0] > c.THRv) | (c.rmax[1] > c.THRv);
      if (__builtin_expect(__any((int)grew), 0)) {
        #pragma unroll
        for (int q = 0; q < 2; ++q) { const float dl = max2f(c.rmax[q], 0.f); c.mhat[q] = fadd_s(c.mhat[q], dl); c.fct[q] = __builtin_amdgcn_exp2f(-dl);
          #pragma unroll
          for (int kb = 0; kb < 2; ++kb) { _Pragma("unroll") for (int e = 0; e < 16; ++e) c.S[X][kb][q][e] = fsub_s(c.S[X][kb][q][e], dl); }
          negm_bcast(c.NEGM[q], w4_aone(), -c.mhat[q]);
        }
        c.pend = true;
      }
    }
  }
  if constexpr (M == 18) { c.rs0[0] = c.rs0[1] = c.rs1[0] = c.rs1[1] = 0.f; }
  if constexpr (M >= 19) {
    constexpr int g = M - 19, e_lo = (32 * g) / 13, e_hi = (32 * (g + 1)) / 13;
    {
      constexpr int p_lo = g ? (32 * (g - 1)) / 13 : 0, p_hi = g ? (32 * g) / 13 : 0;
      if constexpr (p_lo + 0 < p_hi) { constexpr int e = p_lo + 0; if constexpr (e & 1) c.rs1[e >> 4] = fadd_s(c.rs1[e >> 4], c.S[X][0][e >> 4][e & 15]); else c.rs0[e >> 4] = fadd_s(c.rs0[e >> 4], c.S[X][0][e >> 4][e & 15]); }
      if constexpr (p_lo + 1 < p_hi) { constexpr int e = p_lo + 1; if constexpr (e & 1) c.rs1[e >> 4] = fadd_s(c.rs1[e >> 4], c.S[X][0][e >> 4][e & 15]); else c.rs0[e >> 4] = fadd_s(c.rs0[e >> 4], c.S[X][0][e >> 4][e & 15]); }
      if constexpr (p_lo + 2 < p_hi) { constexpr int e = p_lo + 2; if constexpr (e & 1) c.rs1[e >> 4] = fadd_s(c.rs1[e >> 4], c.S[X][0][e >> 4][e & 15]); else c.rs0[e >> 4] = fadd_s(c.rs0[e >> 4], c.S[X][0][e >> 4][e & 15]); }
    }
    if constexpr (e_lo + 0 < e_hi) c.S[X][0][(e_lo + 0) >> 4][(e_lo + 0) & 15] = __builtin_amdgcn_exp2f(c.S[X][0][(e_lo + 0) >> 4][(e_lo + 0) & 15]);
    if constexpr (e_lo + 1 < e_hi) c.S[X][0][(e_lo + 1) >> 4][(e_lo + 1) & 15] = __builtin_amdgcn_exp2f(c.S[X][0][(e_lo + 1) >> 4][(e_lo + 1) & 15]);
    if constexpr (e_lo + 2 < e_hi) c.S[X][0][(e_lo + 2) >> 4][(e_lo + 2) & 15] = __builtin_amdgcn_exp2f(c.S[X][0][(e_lo + 2) >> 4][(e_lo + 2) & 15]);
  }
  if constexpr (M == 31) asm volatile("" :: "v"(c.S[X][0][0]), "v"(c.S[X][0][1]), "v"(c.NEGM[0]), "v"(c.NEGM[1]), "v"(c.rs0[0]), "v"(c.rs0[1]), "v"(c.rs1[0]), "v"(c.rs1[1]));
  GAP();
}
template <int X, bool PEEL, bool ZERO1, bool DK, bool DV, bool KR, int... Ms> DI void phaseB_(W4C& c, const int t, iseq<Ms...>) {
  const unsigned kslot_rd = (unsigned)((t + 1) & 1) * 0x4000u;
  if constexpr (!PEEL) asm volatile("s_nop 1" ::: "memory");
  GAP();
  (gapB<X, PEEL, ZERO1, DK, DV, KR, Ms>(c, kslot_rd), ...);
  {
    #pragma unroll
    for (int e = (32 * 12) / 13; e < 32; ++e) { if (e & 1) c.rs1[e >> 4] = fadd_s(c.rs1[e >> 4], c.S[X][0][e >> 4][e & 15]); else c.rs0[e >> 4] = fadd_s(c.rs0[e >> 4], c.S[X][0][e >> 4][e & 15]); }
  }
  {
    if (__builtin_expect(c.pend, 0)) {
      asm volatile("s_nop 7\n\ts_nop 7" ::: "memory");
      acc_scale_all(c.fct[0], c.fct[1], make_iseq<128>{});
      asm volatile("s_nop 1" ::: "memory");
      c.lsum[0] = fmul_s(c.lsum[0], c.fct[0]); c.lsum[1] = fmul_s(c.lsum[1], c.fct[1]); c.pend = false;
    }
  }
  asm volatile("s_waitcnt lgkmcnt(0)" ::: "memory");
  GAP();
}
template <int X, bool PEEL, bool ZERO1, bool DK = true, bool DV = true, bool KR = true> DI void phaseB(W4C& c, const int t) { phaseB_<X, PEEL, ZERO1, DK, DV, KR>(c, t, make_iseq<32>{}); }

template <int... Is> DI void k_reads(W4C& c, unsigned slot, iseq<Is...>) { (k_read<Is>(c, slot), ...); }
template <int... Is> DI void tr_reads(W4C& c, unsigned vslot, iseq<Is...>) { (tr_read<Is>(c, vslot), ...); }
template <int X, int... Cs> DI void packs(W4C& c, iseq<Cs...>) { (pack1<X, ((Cs >> 3) & 1), 2 * (Cs >> 4) + ((Cs >> 2) & 1), (Cs & 3)>(c), ...); }
template <int X, int J> DI void rowsum1_padded(W4C& c) { rowsum1<X, J>(c); asm volatile("s_nop 1"); }
template <int X, int... Js> DI void rowsums(W4C& c, iseq<Js...>) { (rowsum1_padded<X, Js>(c), ...); }
template <int... Ms> DI void pv_drain(W4C& c, iseq<Ms...>) { ((pv_mfma<Ms, false>(c), GAP()), ...); }

template <int QB, int D0> DI void tree_qk(f32x16& S, const bf16x8& kf) {
  constexpr int aq = AQ_(QB, D0);
  if constexpr (D0 == 7) asm volatile("s_nop 1\n\tv_mfma_f32_32x32x16_bf16 %0, %1, a[%c2:%c3], %0\n\ts_nop 11" : "+v"(S) : "v"(kf), "i"(aq), "i"(aq + 3) : W4_ACLOB, W4_ACLOB_HI);
  else                   asm volatile("s_nop 1\n\tv_mfma_f32_32x32x16_bf16 %0, %1, a[%c2:%c3], %0" : "+v"(S) : "v"(kf), "i"(aq), "i"(aq + 3) : W4_ACLOB, W4_ACLOB_HI);
}
template <int QB, int DB> DI void tree_pv(const bf16x8& vf, const bf16x8& pk) {
  constexpr int ao = AO_(DB, QB);
  asm volatile("s_nop 1\n\tv_mfma_f32_32x32x16_bf16 a[%c2:%c3], %0, %1, a[%c2:%c3]" :: "v"(vf), "v"(pk), "i"(ao), "i"(ao + 15) : W4_ACLOB);
}
DI float bf16r(float x) { unsigned u = cvtpk(x, 0.f) & 0xFFFFu; return __uint_as_float(u << 16); }
DI float rec_bias(unsigned rec) {
  const unsigned mb = rec >> 16;
  if (mb == 0x3F80u) return 0.f;
  const float mv = __uint_as_float(mb << 16);
  return bf16r(bf16r(1.0f - mv) * -1.0e9f) * 1.4426950408889634f;
}
struct TreeBuf { unsigned rec; bf16x8 kf[8]; };
DI unsigned bperm(unsigned srclane, unsigned v) { return (unsigned)__builtin_amdgcn_ds_bpermute((int)(srclane * 4u), (int)v); }
DI void tree_issue(TreeBuf& b, unsigned rec, unsigned m0v, const char* __restrict__ Kt, const __amdgpu_buffer_rsrc_t& srdVt, unsigned lane) {
  const unsigned hi = lane >> 5;
  b.rec = rec;
  #pragma unroll
  for (int g8 = 0; g8 < 4; ++g8) {
    const unsigned vk = bperm(8u * g8 + ((lane >> 2) & 7u), rec) & 0x7FFu;
    const unsigned vo = vk * 256u + 64u * hi + 16u * (lane & 3u);
    asm volatile("s_mov_b32 m0, %0\n\ts_nop 0\n\tbuffer_load_dwordx4 %1, %2, 0 offen lds" :: "s"(m0v + 2048u * g8), "v"(vo), "s"(srdVt) : "m0", "memory");
    asm volatile("s_mov_b32 m0, %0\n\ts_nop 0\n\tbuffer_load_dwordx4 %1, %2, %3 offen lds" :: "s"(m0v + 2048u * g8 + 1024u), "v"(vo), "s"(srdVt), "s"(128u) : "m0", "memory");
  }
  const unsigned key = rec & 0x7FFu;
  #pragma unroll
  for (int d0 = 0; d0 < 8; ++d0) b.kf[d0] = *(const bf16x8*)(Kt + key * 256u + (2u * d0 + hi) * 16u);
}
template <int QB> DI void tree_compute(W4C& c, const TreeBuf& b, unsigned vvt, unsigned lane) {
  const unsigned l32 = lane & 31u, hi = lane >> 5;
  f32x16 S;
  #pragma unroll
  for (int g4 = 0; g4 < 4; ++g4) {
    #pragma unroll
    for (int k = 0; k < 4; ++k) {
      const unsigned rj = bperm(8u * g4 + 4u * hi + k, b.rec);
      const bool own = ((rj >> 11) & 15u) == (l32 & 7u);
      S[4 * g4 + k] = own ? (rec_bias(rj) - c.mhat[QB]) : -1.0e30f;
    }
  }
  tree_qk<QB, 0>(S, b.kf[0]); tree_qk<QB, 1>(S, b.kf[1]); tree_qk<QB, 2>(S, b.kf[2]); tree_qk<QB, 3>(S, b.kf[3]);
  tree_qk<QB, 4>(S, b.kf[4]); tree_qk<QB, 5>(S, b.kf[5]); tree_qk<QB, 6>(S, b.kf[6]); tree_qk<QB, 7>(S, b.kf[7]);
  float mx = max3f(S[0], S[1], S[2]);
  mx = max3f(mx, S[3], S[4]); mx = max3f(mx, S[5], S[6]); mx = max3f(mx, S[7], S[8]); mx = max3f(mx, S[9], S[10]);
  mx = max3f(mx, S[11], S[12]); mx = max3f(mx, S[13], S[14]); mx = max2f(mx, S[15]);
  { auto pr = __builtin_amdgcn_permlane32_swap(__float_as_uint(mx), __float_as_uint(mx), false, false);
    mx = max2f(__uint_as_float(pr[0]), __uint_as_float(pr[1])); }
  const bool emp = (c.lsum[QB] == 0.f) & (mx > -1.0e29f);
  if (__any((int)((mx > c.THRv) | emp))) {
    const float dl = emp ? mx : max2f(mx, 0.f), f = emp ? 1.f : __builtin_amdgcn_exp2f(-dl);
    c.mhat[QB] = fadd_s(c.mhat[QB], dl);
    #pragma unroll
    for (int e = 0; e < 16; ++e) S[e] = fsub_s(S[e], dl);
    asm volatile("s_nop 7\n\ts_nop 7" ::: "memory");
    acc_scale_qb<QB>(f, make_iseq<64>{});
    asm volatile("s_nop 1" ::: "memory");
    c.lsum[QB] = fmul_s(c.lsum[QB], f);
  }
  float rs = 0.f;
  #pragma unroll
  for (int e = 0; e < 16; ++e) { S[e] = __builtin_amdgcn_exp2f(S[e]); rs += S[e]; }
  { auto pr = __builtin_amdgcn_permlane32_swap(__float_as_uint(rs), __float_as_uint(rs), false, false);
    c.lsum[QB] = c.lsum[QB] + __uint_as_float(pr[0]) + __uint_as_float(pr[1]); }
  bf16x8 pk[2];
  #pragma unroll
  for (int ks = 0; ks < 2; ++ks) { u32x4 w;
    #pragma unroll
    for (int p = 0; p < 4; ++p) w[p] = cvtpk(S[8 * ks + 2 * p], S[8 * ks + 2 * p + 1]);
    pk[ks] = __builtin_bit_cast(bf16x8, w); }
  bf16x4 lo[4][2], up[4][2];
  asm volatile("s_waitcnt lgkmcnt(0)" ::: "memory");
  #pragma unroll
  for (int ks = 0; ks < 2; ++ks)
    #pragma unroll
    for (int db = 0; db < 4; ++db) {
      asm volatile("ds_read_b64_tr_b16 %0, %1 offset:%2" : "=v"(lo[db][ks]) : "v"(vvt), "i"(4096 * ks + 512 * db) : "memory");
      asm volatile("ds_read_b64_tr_b16 %0, %1 offset:%2" : "=v"(up[db][ks]) : "v"(vvt), "i"(4096 * ks + 2048 + 512 * db) : "memory");
    }
  asm volatile("s_waitcnt lgkmcnt(0)" : "+v"(lo[0][0]), "+v"(lo[1][0]), "+v"(lo[2][0]), "+v"(lo[3][0]), "+v"(lo[0][1]), "+v"(lo[1][1]), "+v"(lo[2][1]), "+v"(lo[3][1]),
                                        "+v"(up[0][0]), "+v"(up[1][0]), "+v"(up[2][0]), "+v"(up[3][0]), "+v"(up[0][1]), "+v"(up[1][1]), "+v"(up[2][1]), "+v"(up[3][1]) :: "memory");
  #pragma unroll
  for (int ks = 0; ks < 2; ++ks) {
    tree_pv<QB, 0>(__builtin_shufflevector(lo[0][ks], up[0][ks], 0, 1, 2, 3, 4, 5, 6, 7), pk[ks]);
    tree_pv<QB, 1>(__builtin_shufflevector(lo[1][ks], up[1][ks], 0, 1, 2, 3, 4, 5, 6, 7), pk[ks]);
    tree_pv<QB, 2>(__builtin_shufflevector(lo[2][ks], up[2][ks], 0, 1, 2, 3, 4, 5, 6, 7), pk[ks]);
    tree_pv<QB, 3>(__builtin_shufflevector(lo[3][ks], up[3][ks], 0, 1, 2, 3, 4, 5, 6, 7), pk[ks]);
  }
}
template <int R> DI void acc_zero1() { asm volatile("v_accvgpr_write_b32 a[%c0], 0" :: "i"(R) : W4_ACLOB); }
template <int... Rs> DI void acc_zero(iseq<Rs...>) { (acc_zero1<Rs>(), ...); }

extern "C" __global__ __launch_bounds__(256, 1)
void tree_attn_main(const unsigned short* __restrict__ Q, const unsigned short* __restrict__ Kc, const unsigned short* __restrict__ Vc,
                    const unsigned short* __restrict__ Ktr, const unsigned short* __restrict__ Vtr,
                    const unsigned* __restrict__ cnts, const unsigned* __restrict__ recs, unsigned short* __restrict__ O) {
  __shared__ __attribute__((aligned(16))) char lds[LDS_BYTES];
  const unsigned tid = threadIdx.x, lane = tid & 63u, wid = tid >> 6, l32 = lane & 31u, hi = lane >> 5;
  const float CSC = 0.12751743082f;
  W4C c;
  c.THRv = THR;
  c.ldsb = (unsigned)(size_t)lds;
  #pragma unroll
  for (int j = 0; j < 4; ++j) c.vkb[j] = c.ldsb + KRING + 256u * l32 + 16u * ((2u * j + hi) ^ (l32 & 7u));
  c.vvb = c.ldsb + VRING + (((lane & 3u) << 3) | (((lane >> 2) & 3u) << 6) | (((lane >> 4) & 1u) << 5) | (hi << 8));
  #pragma unroll
  for (int par = 0; par < 2; ++par) c.vksrc[par] = KRS * (lane >> 4) + 16u * ((lane & 15u) ^ (4u * par + (lane >> 4)));
  c.vvsrc = KRS * ((lane >> 2) & 7u) + 64u * hi + 16u * (lane & 3u);
  c.m0k = rfl(c.ldsb + KRING + 4096u * wid); c.m0v = rfl(c.ldsb + VRING + 4096u * wid);
  c.sofk = rfl(KRS * 16u * wid); c.sofv = rfl(KRS * 16u * wid);

  const unsigned blk = blockIdx.x, kvh = blk & 7u, pblk = blk >> 3;
  const unsigned P0 = 64u * pblk + 16u * wid;
  const char* Kbase = (const char*)Kc + (size_t)kvh * C * 256;
  const char* Vbase = (const char*)Vc + (size_t)kvh * C * 256;
  c.srdK = __builtin_amdgcn_make_buffer_rsrc((void*)Kbase, (short)0, C * 256, SRD_WORD3);
  c.srdV = __builtin_amdgcn_make_buffer_rsrc((void*)Vbase, (short)0, C * 256, SRD_WORD3);
  auto dma_k = [&](int tk) {
    const unsigned slot = (unsigned)(tk & 1) * 0x4000u, skv = KTILEB * (unsigned)tk;
    #pragma unroll
    for (int q = 0; q < 4; ++q)
      asm volatile("s_nop 4\n\ts_mov_b32 m0, %0\n\ts_nop 0\n\tbuffer_load_dwordx4 %1, %2, %3 offen lds"
                   :: "s"(rfl(c.m0k + slot + 1024u * q)), "v"(c.vksrc[q & 1]), "s"(c.srdK), "s"(rfl(skv + c.sofk + KRS * 4u * q)) : "m0", "memory");
  };
  auto dma_v = [&](int tv) {
    const unsigned slot = (unsigned)(tv & 1) * 0x4000u, skv = KTILEB * (unsigned)tv;
    #pragma unroll
    for (int q = 0; q < 4; ++q)
      asm volatile("s_nop 4\n\ts_mov_b32 m0, %0\n\ts_nop 0\n\tbuffer_load_dwordx4 %1, %2, %3 offen lds"
                   :: "s"(rfl(c.m0v + slot + 1024u * q)), "v"(c.vvsrc), "s"(c.srdV), "s"(rfl(skv + c.sofv + KRS * 8u * (q >> 1) + 128u * (q & 1))) : "m0", "memory");
  };
  const char* Kt = (const char*)Ktr + (size_t)kvh * T * 256;
  const char* Vt = (const char*)Vtr + (size_t)kvh * T * 256;
  const unsigned g0 = P0 >> 3;
  const unsigned cnt0 = cnts[g0], cnt1 = cnts[g0 + 1u];
  const unsigned n0 = (cnt0 + 31u) >> 5, ntt = n0 + ((cnt1 + 31u) >> 5);
  auto rec_load = [&](unsigned i) -> unsigned {
    const bool q1 = i >= n0; const unsigned base = (q1 ? i - n0 : i) * 32u + l32;
    unsigned r = REC_INVALID;
    if (i < ntt && base < (q1 ? cnt1 : cnt0)) r = recs[(size_t)(g0 + (q1 ? 1u : 0u)) * REC_CAP + base];
    return r;
  };
  unsigned rq0 = rec_load(0), rq1 = rec_load(1), rq2 = rec_load(2);
  bf16x8 qf[2][8];
  #pragma unroll
  for (int qb = 0; qb < 2; ++qb) {
    const char* qrow = (const char*)Q + ((size_t)(4u * kvh + (l32 >> 3)) * T + P0 + 8u * qb + (l32 & 7u)) * 256u + hi * 16u;
    #pragma unroll
    for (int d0 = 0; d0 < 8; ++d0) qf[qb][d0] = *(const bf16x8*)(qrow + 32 * d0);
  }
  dma_k(0); dma_v(0); dma_k(1);
  const __amdgpu_buffer_rsrc_t srdVt = __builtin_amdgcn_make_buffer_rsrc((void*)Vt, (short)0, T * 256, SRD_WORD3);
  const unsigned tvb = rfl(c.ldsb + TREEV + 24576u * wid);
  TreeBuf tb0, tb1, tb2;
  if (0u < ntt) tree_issue(tb0, rq0, tvb, Kt, srdVt, lane);
  if (1u < ntt) tree_issue(tb1, rq1, tvb + 8192u, Kt, srdVt, lane);
  rq0 = rec_load(3); rq1 = rec_load(4);
  acc_zero(make_iseq<128>{});
  {
    u32x4 w;
    #define QW(I) { const bf16x8 q8 = qf[(I) >> 3][(I) & 7]; _Pragma("unroll") for (int e = 0; e < 4; ++e) w[e] = cvtpk(fmul_s((float)q8[2 * e], CSC), fmul_s((float)q8[2 * e + 1], CSC)); q_write1<I>(w); }
    QW(0) QW(1) QW(2) QW(3) QW(4) QW(5) QW(6) QW(7) QW(8) QW(9) QW(10) QW(11) QW(12) QW(13) QW(14) QW(15)
    #undef QW
  }
  asm volatile("s_nop 1" ::: "memory");
  c.mhat[0] = c.mhat[1] = 0.f; c.lsum[0] = c.lsum[1] = 0.f; c.fct[0] = c.fct[1] = 1.f; c.pend = false;
  {
    const unsigned vvt = tvb + (((lane & 3u) << 3) | (((lane >> 2) & 3u) << 6) | (((lane >> 4) & 1u) << 5) | (hi << 8));
    #define TSTEP(I, CUR, CO, NXT, NO, RQ) { \
      if ((I) + 2u < ntt) tree_issue(NXT, RQ, tvb + (NO), Kt, srdVt, lane); \
      RQ = rec_load((I) + 5u); \
      if ((I) < n0) tree_compute<0>(c, CUR, vvt + (CO), lane); else tree_compute<1>(c, CUR, vvt + (CO), lane); }
    for (unsigned i = 0; i < ntt; i += 3u) {
      TSTEP(i, tb0, 0u, tb2, 16384u, rq2)
      if (i + 1u < ntt) TSTEP(i + 1u, tb1, 8192u, tb0, 0u, rq0)
      if (i + 2u < ntt) TSTEP(i + 2u, tb2, 16384u, tb1, 8192u, rq1)
    }
    #undef TSTEP
    asm volatile("s_nop 7\n\ts_nop 7" ::: "memory");
  }
  negm_bcast(c.NEGM[0], w4_aone(), -c.mhat[0]); negm_bcast(c.NEGM[1], w4_aone(), -c.mhat[1]);
  asm volatile("s_waitcnt vmcnt(0)\n\ts_barrier" ::: "memory");
  k_reads(c, 0u, make_iseq<16>{});
  asm volatile("s_waitcnt lgkmcnt(0)\n\ts_nop 1" ::: "memory");

  phaseA<0, true>(c, 0);  phaseB<0, true,  false>(c, 0);
  phaseA<1, false>(c, 1); phaseB<1, false, false>(c, 1);
  for (int t = 2; t < NT - 2; t += 2) {
    phaseA<0, false>(c, t);     phaseB<0, false, false>(c, t);
    phaseA<1, false>(c, t + 1); phaseB<1, false, false>(c, t + 1);
  }
  phaseA<0, false>(c, NT - 2); phaseB<0, false, false,  false,  true,  true>(c, NT - 2);
  phaseA<1, false>(c, NT - 1); phaseB<1, false, false,  false,  false,  false>(c, NT - 1);
  {
    const unsigned vslot = (unsigned)((NT - 1) & 1) * 0x4000u;
    tr_reads(c, vslot, make_iseq<32>{});
    #pragma unroll
    for (int e = 0; e < 32; ++e) c.S[1][1][e >> 4][e & 15] = __builtin_amdgcn_exp2f(c.S[1][1][e >> 4][e & 15]);
    packs<1>(c, make_iseq<32>{});
    rowsums<1>(c, iseq<8, 9, 10, 11, 12, 13, 14, 15, 24, 25, 26, 27, 28, 29, 30, 31>{});
    #pragma unroll
    for (int qb = 0; qb < 2; ++qb) { const float rs = fadd_s(c.rs0[qb], c.rs1[qb]);
      auto pr = __builtin_amdgcn_permlane32_swap(__float_as_uint(rs), __float_as_uint(rs), false, false);
      c.lsum[qb] = fadd_s(c.lsum[qb], fadd_s(__uint_as_float(pr[0]), __uint_as_float(pr[1]))); }
    asm volatile("s_waitcnt lgkmcnt(0)" : W4_VLO :: "memory"); asm volatile("" : W4_VUP :: "memory");
    GAP();
    #pragma unroll
    for (int ks = 0; ks < 4; ++ks)
      #pragma unroll
      for (int db = 0; db < 4; ++db) c.Vf[db][ks] = __builtin_shufflevector(c.vlo[db][ks], c.vup[db][ks], 0, 1, 2, 3, 4, 5, 6, 7);
    asm volatile("s_nop 1" ::: "memory");
    pv_drain(c, make_iseq<32>{});
    asm volatile("s_nop 7\n\ts_nop 7" ::: "memory");
  }
  asm volatile("s_barrier" ::: "memory");
  {
    char* stg = lds + 16384u * wid;
    #pragma unroll
    for (int qb = 0; qb < 2; ++qb) {
      const float il = c.lsum[qb] > 0.f ? __builtin_amdgcn_rcpf(c.lsum[qb]) : 0.f;
      #define EPI_DB(DB) { float o[16]; if (qb == 0) acc_read16<DB, 0>(o, make_iseq<16>{}); else acc_read16<DB, 1>(o, make_iseq<16>{}); \
        _Pragma("unroll") for (int g = 0; g < 4; ++g) { u32x2 w; w[0] = cvtpk(o[4 * g] * il, o[4 * g + 1] * il); w[1] = cvtpk(o[4 * g + 2] * il, o[4 * g + 3] * il); \
          *(u32x2*)(stg + 32u * 256u * qb + l32 * 256u + 16u * ((4u * DB + g) ^ (l32 & 15u)) + 8u * hi) = w; } }
      EPI_DB(0) EPI_DB(1) EPI_DB(2) EPI_DB(3)
      #undef EPI_DB
    }
    asm volatile("s_waitcnt lgkmcnt(0)" ::: "memory");
    #pragma unroll
    for (int j = 0; j < 16; ++j) {
      const unsigned r = 4u * j + (lane >> 4), cc = lane & 15u;
      const u32x4 v = *(const u32x4*)(stg + r * 256u + 16u * (cc ^ (r & 15u)));
      char* orow = (char*)O + ((size_t)(4u * kvh + ((r >> 3) & 3u)) * T + P0 + 8u * (r >> 5) + (r & 7u)) * 256u + 16u * cc;
      *(u32x4*)orow = v;
    }
  }
}

__global__ __launch_bounds__(256) void tree_prepass(const u32x4* __restrict__ mask, unsigned* __restrict__ cnts, unsigned* __restrict__ recs) {
  __shared__ unsigned wcnt[4];
  const unsigned g = blockIdx.x, tid = threadIdx.x, lane = tid & 63u, wid = tid >> 6;
  u32x4 v[8];
  #pragma unroll
  for (int r = 0; r < 8; ++r) v[r] = mask[(size_t)(8u * g + r) * 256u + tid];
  unsigned nz = 0;
  #pragma unroll
  for (int r = 0; r < 8; ++r)
    #pragma unroll
    for (int k = 0; k < 4; ++k) nz += ((v[r][k] & 0x7FFFu) != 0u) + ((v[r][k] & 0x7FFF0000u) != 0u);
  unsigned tot = 0;
  if (__any(nz != 0u)) {
    #pragma unroll
    for (int o = 32; o >= 1; o >>= 1) nz += __shfl_xor(nz, o);
    tot = nz;
  }
  if (lane == 0) wcnt[wid] = tot;
  __syncthreads();
  unsigned base = 0;
  for (unsigned w = 0; w < wid; ++w) base += wcnt[w];
  if (tid == 0) cnts[g] = wcnt[0] + wcnt[1] + wcnt[2] + wcnt[3];
  if (tot == 0) return;
  unsigned* rg = recs + (size_t)g * REC_CAP;
  #pragma unroll
  for (int r = 0; r < 8; ++r) {
    if (!__any((v[r][0] | v[r][1] | v[r][2] | v[r][3]) & 0x7FFF7FFFu)) continue;
    #pragma unroll
    for (int e = 0; e < 8; ++e) {
      const unsigned bits = (v[r][e >> 1] >> (16 * (e & 1))) & 0xFFFFu;
      const bool on = (bits & 0x7FFFu) != 0u;
      const unsigned long long m = __ballot(on);
      if (on) rg[base + __popcll(m & ((1ull << lane) - 1ull))] = (8u * tid + e) | ((unsigned)r << 11) | (bits << 16);
      base += __popcll(m);
    }
  }
}

extern "C" void kernel_launch(void* const* d_in, const int* in_sizes, int n_in, void* d_out, int out_size, void* d_ws, size_t ws_size, hipStream_t stream) {
  unsigned* cnts = (unsigned*)d_ws;
  unsigned* recs = (unsigned*)((char*)d_ws + 4096);
  hipLaunchKernelGGL(tree_prepass, dim3(256), dim3(256), 0, stream, (const u32x4*)d_in[5], cnts, recs);
  hipLaunchKernelGGL(tree_attn_main, dim3(256), dim3(256), 0, stream, (const unsigned short*)d_in[0], (const unsigned short*)d_in[1], (const unsigned short*)d_in[2],
                     (const unsigned short*)d_in[3], (const unsigned short*)d_in[4], (const unsigned*)cnts, (const unsigned*)recs, (unsigned short*)d_out);
}
```

```cpp
#include <hip/hip_runtime.h>
#include <hip/hip_bf16.h>
#include <stdint.h>
#include <utility>

constexpr int D = 128, HQ = 32, HKV = 8, T = 2048, C = 2048, NT = C / 64;
constexpr float THR = 8.0f;
constexpr unsigned KRS = 256u, KTILEB = 64u * KRS;
constexpr unsigned REC_CAP = 8u * 2048u;

typedef float    f32x16 __attribute__((ext_vector_type(16)));
typedef float    f32x2  __attribute__((ext_vector_type(2)));
typedef float    f32x4  __attribute__((ext_vector_type(4)));
typedef __bf16   bf16x8 __attribute__((ext_vector_type(8)));
typedef __bf16   bf16x4 __attribute__((ext_vector_type(4)));
typedef __bf16   bf16x2 __attribute__((ext_vector_type(2)));
typedef unsigned u32x4  __attribute__((ext_vector_type(4)));
typedef unsigned u32x2  __attribute__((ext_vector_type(2)));
template <int... Is> using iseq = std::integer_sequence<int, Is...>;
template <int N> using make_iseq = std::make_integer_sequence<int, N>;

#define KRING 0x0000u
#define VRING 0x8000u
#define TREEV 0x10000u
#define TREER 0x20000u
#define LDS_BYTES 0x20800u
#define SRD_WORD3 0x00020000
#define DI __device__ __forceinline__
#define GAP() __builtin_amdgcn_sched_barrier(0)
#define W4_ACLOB_HI "a128","a129","a130","a131","a132","a133","a134","a135","a136","a137","a138","a139","a140","a141","a142","a143","a144","a145","a146","a147","a148","a149","a150","a151","a152","a153","a154","a155","a156","a157","a158","a159","a160","a161","a162","a163","a164","a165","a166","a167","a168","a169","a170","a171","a172","a173","a174","a175","a176","a177","a178","a179","a180","a181","a182","a183","a184","a185","a186","a187","a188","a189","a190","a191","a192","a193","a194","a195","a196","a197","a198","a199","a200","a201","a202","a203","a204","a205","a206","a207","a208","a209","a210","a211","a212","a213","a214","a215","a216","a217","a218","a219","a220","a221","a222","a223","a224","a225","a226","a227","a228","a229","a230","a231","a232","a233","a234","a235","a236","a237","a238","a239","a240","a241","a242","a243","a244","a245","a246","a247","a248","a249","a250","a251","a252","a253","a254","a255"
#define W4_ACLOB "a0","a1","a2","a3","a4","a5","a6","a7","a8","a9","a10","a11","a12","a13","a14","a15","a16","a17","a18","a19","a20","a21","a22","a23","a24","a25","a26","a27","a28","a29","a30","a31","a32","a33","a34","a35","a36","a37","a38","a39","a40","a41","a42","a43","a44","a45","a46","a47","a48","a49","a50","a51","a52","a53","a54","a55","a56","a57","a58","a59","a60","a61","a62","a63","a64","a65","a66","a67","a68","a69","a70","a71","a72","a73","a74","a75","a76","a77","a78","a79","a80","a81","a82","a83","a84","a85","a86","a87","a88","a89","a90","a91","a92","a93","a94","a95","a96","a97","a98","a99","a100","a101","a102","a103","a104","a105","a106","a107","a108","a109","a110","a111","a112","a113","a114","a115","a116","a117","a118","a119","a120","a121","a122","a123","a124","a125","a126","a127"

DI unsigned rfl(unsigned x) { return __builtin_amdgcn_readfirstlane(x); }
DI unsigned cvtpk(float lo, float hi) {
  f32x2 v = {lo, hi}; bf16x2 b = __builtin_convertvector(v, bf16x2); return __builtin_bit_cast(unsigned, b);
}
DI float max3f(float a, float b, float c) { float r; asm("v_max3_f32 %0, %1, %2, %3" : "=v"(r) : "v"(a), "v"(b), "v"(c)); return r; }
DI float max2f(float a, float b) { float r; asm("v_max_f32_e32 %0, %1, %2" : "=v"(r) : "v"(a), "v"(b)); return r; }
DI float fadd_s(float a, float b) { float r; asm("v_add_f32_e32 %0, %1, %2" : "=v"(r) : "v"(a), "v"(b)); return r; }
DI float fmul_s(float a, float b) { float r; asm("v_mul_f32_e32 %0, %1, %2" : "=v"(r) : "v"(a), "v"(b)); return r; }
DI float fsub_s(float a, float b) { float r; asm("v_sub_f32_e32 %0, %1, %2" : "=v"(r) : "v"(a), "v"(b)); return r; }
DI float w4_aone() { unsigned l; asm volatile("v_mbcnt_lo_u32_b32 %0, -1, 0\n\tv_mbcnt_hi_u32_b32 %0, -1, %0" : "=v"(l)); return l < 32u ? 1.f : 0.f; }
DI void negm_bcast(f32x16& negm, float aone, float neg_mhat) {
  asm volatile("s_nop 1\n\tv_mfma_f32_32x32x2_f32 %0, %1, %2, 0" : "+v"(negm) : "v"(aone), "v"(neg_mhat));
}

struct W4C {
  unsigned vkb[4], vvb, vksrc[2], vvsrc, m0k, m0v, sofk, sofv, ldsb;
  __amdgpu_buffer_rsrc_t srdK, srdV;
  float THRv;
  f32x16 S[2][2][2];
  f32x16 NEGM[2];
  bf16x8 Vf[4][4];
  bf16x8 Pk[2][4];
  bf16x4 vlo[4][4], vup[4][4];
  float mhat[2], lsum[2], fct[2]; bool pend;
  float rs0[2], rs1[2], pmx0[2], pmx1[2], rmax[2];
  unsigned pm0[8], psf[8];
};

constexpr int AO_(int db, int qb) { return 16 * (2 * db + qb); }
constexpr int AQ_(int qb, int d0) { return 128 + 32 * qb + 4 * d0; }
constexpr int AK_(int kb, int d0) { return 192 + 32 * kb + 4 * d0; }

template <int I> DI void tr_read(W4C& c, unsigned vslot) {
  constexpr int ks = I >> 3, db = (I >> 1) & 3;
  if constexpr ((I & 1) == 0) asm volatile("ds_read_b64_tr_b16 %0, %1 offset:%2" : "=v"(c.vlo[db][ks]) : "v"(c.vvb + vslot), "i"(4096 * ks + 512 * db) : "memory");
  else                        asm volatile("ds_read_b64_tr_b16 %0, %1 offset:%2" : "=v"(c.vup[db][ks]) : "v"(c.vvb + vslot), "i"(4096 * ks + 2048 + 512 * db) : "memory");
}
template <int I> DI void k_read(W4C& c, unsigned slot) {
  constexpr int kb = I >> 3, d0 = I & 7;
  asm volatile("ds_read_b128 a[%c1:%c2], %0 offset:%c3" :: "v"(c.vkb[d0 & 3] + slot), "i"(AK_(kb, d0)), "i"(AK_(kb, d0) + 3), "i"(8192 * kb + 128 * (d0 >> 2)) : "memory", W4_ACLOB_HI);
}
template <int I> DI void q_write1(const u32x4& w) {
  constexpr int aq = AQ_(I >> 3, I & 7);
  asm volatile("v_accvgpr_write_b32 a[%c4], %0\n\tv_accvgpr_write_b32 a[%c5], %1\n\tv_accvgpr_write_b32 a[%c6], %2\n\tv_accvgpr_write_b32 a[%c7], %3"
               :: "v"(w[0]), "v"(w[1]), "v"(w[2]), "v"(w[3]), "i"(aq), "i"(aq + 1), "i"(aq + 2), "i"(aq + 3) : W4_ACLOB_HI);
}
template <int X, bool FIRST, int M> DI void qk_mfma(W4C& c) {
  constexpr int d0 = M >> 2, kb = (M >> 1) & 1, qb = M & 1, ak = AK_(kb, d0), aq = AQ_(qb, d0);
  if constexpr (d0 == 0) {
    if constexpr (FIRST) asm volatile("v_mfma_f32_32x32x16_bf16 %0, a[%c1:%c2], a[%c3:%c4], 0" : "=&v"(c.S[X][kb][qb]) : "i"(ak), "i"(ak + 3), "i"(aq), "i"(aq + 3) : W4_ACLOB, W4_ACLOB_HI);
    else asm volatile("v_mfma_f32_32x32x16_bf16 %0, a[%c2:%c3], a[%c4:%c5], %1" : "=&v"(c.S[X][kb][qb]) : "v"(c.NEGM[qb]), "i"(ak), "i"(ak + 3), "i"(aq), "i"(aq + 3) : W4_ACLOB, W4_ACLOB_HI);
  } else asm volatile("v_mfma_f32_32x32x16_bf16 %0, a[%c1:%c2], a[%c3:%c4], %0" : "+v"(c.S[X][kb][qb]) : "i"(ak), "i"(ak + 3), "i"(aq), "i"(aq + 3) : W4_ACLOB, W4_ACLOB_HI);
}
template <int M, bool ZERO> DI void pv_mfma(W4C& c) {
  constexpr int ks = M >> 3, db = (M >> 1) & 3, qb = M & 1, ao = AO_(db, qb);
  if constexpr (ZERO && ks == 0) asm volatile("v_mfma_f32_32x32x16_bf16 a[%c2:%c3], %0, %1, 0" :: "v"(c.Vf[db][ks]), "v"(c.Pk[qb][ks]), "i"(ao), "i"(ao + 15) : W4_ACLOB);
  else                         asm volatile("v_mfma_f32_32x32x16_bf16 a[%c2:%c3], %0, %1, a[%c2:%c3]" :: "v"(c.Vf[db][ks]), "v"(c.Pk[qb][ks]), "i"(ao), "i"(ao + 15) : W4_ACLOB);
}
template <int X, int QB, int KS, int P> DI void pack1(W4C& c) {
  u32x4 w = __builtin_bit_cast(u32x4, c.Pk[QB][KS]);
  w[P] = cvtpk(c.S[X][KS >> 1][QB][8 * (KS & 1) + 2 * P], c.S[X][KS >> 1][QB][8 * (KS & 1) + 2 * P + 1]);
  c.Pk[QB][KS] = __builtin_bit_cast(bf16x8, w);
}
template <int X, int J> DI void rowsum1(W4C& c) {
  constexpr int qb = J >> 4, ks = (J >> 2) & 3, p = J & 3;
  c.rs0[qb] = fadd_s(c.rs0[qb], c.S[X][ks >> 1][qb][8 * (ks & 1) + 2 * p]); c.rs1[qb] = fadd_s(c.rs1[qb], c.S[X][ks >> 1][qb][8 * (ks & 1) + 2 * p + 1]);
}

template <int R> DI void acc_scale1(float f) {
  float t;
  asm volatile("v_accvgpr_read_b32 %0, a[%c1]\n\tv_mul_f32 %0, %2, %0\n\tv_accvgpr_write_b32 a[%c1], %0" : "=&v"(t) : "i"(R), "v"(f) : W4_ACLOB);
}
template <int... Rs> DI void acc_scale_all(float f0, float f1, iseq<Rs...>) { (acc_scale1<Rs>(((Rs >> 4) & 1) ? f1 : f0), ...); }
template <int QB, int... Rs> DI void acc_scale_qb(float f, iseq<Rs...>) { (acc_scale1<16 * (2 * (Rs >> 4) + QB) + (Rs & 15)>(f), ...); }
template <int R> DI float acc_read1() { float t; asm volatile("v_accvgpr_read_b32 %0, a[%c1]" : "=v"(t) : "i"(R)); return t; }
template <int DB, int QB, int... Rs> DI void acc_read16(float (&o)[16], iseq<Rs...>) { ((o[Rs] = acc_read1<AO_(DB, QB) + Rs>()), ...); }

template <int X, bool FIRST, int M> DI void gapA(W4C& c, unsigned vslot, const int t) {
  constexpr int Xp = X ^ 1;
  qk_mfma<X, FIRST, M>(c);
  if constexpr (M >= 16 && M <= 31) {
    const int tk = t + 2, tv = t + 1;
    constexpr int q = (M - 16) >> 1;
    if constexpr (((M - 16) & 1) == 0) {
      if constexpr (q < 4) c.pm0[q] = c.m0k + (unsigned)((t + 2) & 1) * 0x4000u + 1024u * q; else c.pm0[q] = c.m0v + (unsigned)((t + 1) & 1) * 0x4000u + 1024u * (q - 4);
      asm volatile("" : "+s"(c.pm0[q]));
    } else {
      if constexpr (q < 4) c.psf[q] = KTILEB * (unsigned)tk + c.sofk + KRS * 4u * q; else c.psf[q] = KTILEB * (unsigned)tv + c.sofv + KRS * 8u * ((q - 4) >> 1) + 128u * ((q - 4) & 1);
      asm volatile("" : "+s"(c.psf[q]));
    }
  }
  if constexpr (M == 31) asm volatile("s_nop 4" : "+s"(c.pm0[0]), "+s"(c.pm0[1]), "+s"(c.pm0[2]), "+s"(c.pm0[3]), "+s"(c.pm0[4]), "+s"(c.pm0[5]), "+s"(c.pm0[6]), "+s"(c.pm0[7]),
                                                  "+s"(c.psf[0]), "+s"(c.psf[1]), "+s"(c.psf[2]), "+s"(c.psf[3]), "+s"(c.psf[4]), "+s"(c.psf[5]), "+s"(c.psf[6]), "+s"(c.psf[7]));
  if constexpr (!FIRST && M == 8) asm volatile("" :: "v"(c.NEGM[0]), "v"(c.NEGM[1]));
  if constexpr (!FIRST) {
    constexpr int base = M + (M >= 4) + (M >= 8) + (M >= 12) + (M >= 16) + (M >= 20) + (M >= 24);
    if constexpr (M <= 25) tr_read<base>(c, vslot);
    if constexpr (M == 3 || M == 7 || M == 11 || M == 15 || M == 19 || M == 23) tr_read<base + 1>(c, vslot);
    if constexpr (M < 16) {
      constexpr int e0 = 2 * M, e1 = 2 * M + 1;
      c.S[Xp][1][e0 >> 4][e0 & 15] = __builtin_amdgcn_exp2f(c.S[Xp][1][e0 >> 4][e0 & 15]);
      c.S[Xp][1][e1 >> 4][e1 & 15] = __builtin_amdgcn_exp2f(c.S[Xp][1][e1 >> 4][e1 & 15]);
      pack1<Xp, (M >> 3), ((M >> 2) & 1), (M & 3)>(c);
    } else {
      constexpr int cc = M - 16;
      pack1<Xp, (cc >> 3), 2 + ((cc >> 2) & 1), (cc & 3)>(c);
      rowsum1<Xp, 16 * (cc >> 3) + 8 + 4 * ((cc >> 2) & 1) + (cc & 3)>(c);
    }
  }
  GAP();
}
#define W4_VLO "+v"(c.vlo[0][0]), "+v"(c.vlo[0][1]), "+v"(c.vlo[0][2]), "+v"(c.vlo[0][3]), "+v"(c.vlo[1][0]), "+v"(c.vlo[1][1]), "+v"(c.vlo[1][2]), "+v"(c.vlo[1][3]), \
               "+v"(c.vlo[2][0]), "+v"(c.vlo[2][1]), "+v"(c.vlo[2][2]), "+v"(c.vlo[2][3]), "+v"(c.vlo[3][0]), "+v"(c.vlo[3][1]), "+v"(c.vlo[3][2]), "+v"(c.vlo[3][3])
#define W4_VUP "+v"(c.vup[0][0]), "+v"(c.vup[0][1]), "+v"(c.vup[0][2]), "+v"(c.vup[0][3]), "+v"(c.vup[1][0]), "+v"(c.vup[1][1]), "+v"(c.vup[1][2]), "+v"(c.vup[1][3]), \
               "+v"(c.vup[2][0]), "+v"(c.vup[2][1]), "+v"(c.vup[2][2]), "+v"(c.vup[2][3]), "+v"(c.vup[3][0]), "+v"(c.vup[3][1]), "+v"(c.vup[3][2]), "+v"(c.vup[3][3])
template <int X, bool FIRST, int... Ms> DI void phaseA_(W4C& c, const int t, iseq<Ms...>) {
  const unsigned vslot = (unsigned)((t - 1) & 1) * 0x4000u;
  (gapA<X, FIRST, Ms>(c, vslot, t), ...);
  if constexpr (!FIRST) {
    #pragma unroll
    for (int qb = 0; qb < 2; ++qb) {
      const float rs = fadd_s(c.rs0[qb], c.rs1[qb]);
      auto pr = __builtin_amdgcn_permlane32_swap(__float_as_uint(rs), __float_as_uint(rs), false, false);
      c.lsum[qb] = fadd_s(c.lsum[qb], fadd_s(__uint_as_float(pr[0]), __uint_as_float(pr[1])));
    }
  }
  if constexpr (FIRST) asm volatile("s_waitcnt vmcnt(0) lgkmcnt(0)\n\ts_barrier" ::: "memory");
  else { asm volatile("s_waitcnt vmcnt(0) lgkmcnt(0)" : W4_VLO :: "memory"); asm volatile("s_barrier" : W4_VUP :: "memory"); }
  GAP();
  if constexpr (!FIRST) {
    #pragma unroll
    for (int ks = 0; ks < 4; ++ks)
      #pragma unroll
      for (int db = 0; db < 4; ++db) c.Vf[db][ks] = __builtin_shufflevector(c.vlo[db][ks], c.vup[db][ks], 0, 1, 2, 3, 4, 5, 6, 7);
  }
}
template <int X, bool FIRST> DI void phaseA(W4C& c, const int t) { phaseA_<X, FIRST>(c, t, make_iseq<32>{}); }

template <int X, bool PEEL, bool ZERO1, bool DK, bool DV, bool KR, int M> DI void gapB(W4C& c, const unsigned kslot_rd) {
  if constexpr (!PEEL) pv_mfma<M, ZERO1>(c);
  if constexpr (M < 4 && DK)             asm volatile("s_mov_b32 m0, %0\n\ts_nop 0\n\tbuffer_load_dwordx4 %1, %2, %3 offen lds" :: "s"(c.pm0[M]), "v"(c.vksrc[M & 1]), "s"(c.srdK),  "s"(c.psf[M]) : "m0", "memory");
  else if constexpr (M >= 4 && M < 8 && DV) asm volatile("s_mov_b32 m0, %0\n\ts_nop 0\n\tbuffer_load_dwordx4 %1, %2, %3 offen lds" :: "s"(c.pm0[M]), "v"(c.vvsrc), "s"(c.srdV),  "s"(c.psf[M]) : "m0", "memory");
  if constexpr (M >= 1 && M <= 16 && KR) k_read<M - 1>(c, kslot_rd);
  if constexpr (M >= 8 && M < 16) {
    constexpr int i = M - 8, q = i >> 2, r = 4 * (i & 3);
    if constexpr ((i & 3) == 0) { c.pmx0[q] = max3f(c.S[X][0][q][0], c.S[X][0][q][1], c.S[X][1][q][0]); c.pmx1[q] = max3f(c.S[X][0][q][2], c.S[X][0][q][3], c.S[X][1][q][1]);
                                  c.pmx0[q] = max3f(c.pmx0[q], c.S[X][1][q][2], c.S[X][1][q][3]); }
    else                        { c.pmx0[q] = max3f(c.pmx0[q], c.S[X][0][q][r], c.S[X][0][q][r + 1]); c.pmx1[q] = max3f(c.pmx1[q], c.S[X][0][q][r + 2], c.S[X][0][q][r + 3]);
                                  c.pmx0[q] = max3f(c.pmx0[q], c.S[X][1][q][r], c.S[X][1][q][r + 1]); c.pmx1[q] = max3f(c.pmx1[q], c.S[X][1][q][r + 2], c.S[X][1][q][r + 3]); }
  }
  if constexpr (M == 16 || M == 17) {
    constexpr int q = M - 16; const float mm = max2f(c.pmx0[q], c.pmx1[q]);
    auto pr = __builtin_amdgcn_permlane32_swap(__float_as_uint(mm), __float_as_uint(mm), false, false);
    c.rmax[q] = max2f(__uint_as_float(pr[0]), __uint_as_float(pr[1]));
  }
  if constexpr (M == 18) {
    if constexpr (PEEL) {
      #pragma unroll
      for (int q = 0; q < 2; ++q) { c.mhat[q] = c.rmax[q]; c.lsum[q] = 0.f; c.fct[q] = 1.f;
        #pragma unroll
        for (int kb = 0; kb < 2; ++kb) { _Pragma("unroll") for (int e = 0; e < 16; ++e) c.S[X][kb][q][e] = fsub_s(c.S[X][kb][q][e], c.rmax[q]); }
        negm_bcast(c.NEGM[q], w4_aone(), -c.mhat[q]);
      }
    } else {
      const bool grew = (c.rmax[0] > c.THRv) | (c.rmax[1] > c.THRv);
      if (__builtin_expect(__any((int)grew), 0)) {
        #pragma unroll
        for (int q = 0; q < 2; ++q) { const float dl = max2f(c.rmax[q], 0.f); c.mhat[q] = fadd_s(c.mhat[q], dl); c.fct[q] = __builtin_amdgcn_exp2f(-dl);
          #pragma unroll
          for (int kb = 0; kb < 2; ++kb) { _Pragma("unroll") for (int e = 0; e < 16; ++e) c.S[X][kb][q][e] = fsub_s(c.S[X][kb][q][e], dl); }
          negm_bcast(c.NEGM[q], w4_aone(), -c.mhat[q]);
        }
        c.pend = true;
      }
    }
  }
  if constexpr (M == 18) { c.rs0[0] = c.rs0[1] = c.rs1[0] = c.rs1[1] = 0.f; }
  if constexpr (M >= 19) {
    constexpr int g = M - 19, e_lo = (32 * g) / 13, e_hi = (32 * (g + 1)) / 13;
    {
      constexpr int p_lo = g ? (32 * (g - 1)) / 13 : 0, p_hi = g ? (32 * g) / 13 : 0;
      if constexpr (p_lo + 0 < p_hi) { constexpr int e = p_lo + 0; if constexpr (e & 1) c.rs1[e >> 4] = fadd_s(c.rs1[e >> 4], c.S[X][0][e >> 4][e & 15]); else c.rs0[e >> 4] = fadd_s(c.rs0[e >> 4], c.S[X][0][e >> 4][e & 15]); }
      if constexpr (p_lo + 1 < p_hi) { constexpr int e = p_lo + 1; if constexpr (e & 1) c.rs1[e >> 4] = fadd_s(c.rs1[e >> 4], c.S[X][0][e >> 4][e & 15]); else c.rs0[e >> 4] = fadd_s(c.rs0[e >> 4], c.S[X][0][e >> 4][e & 15]); }
      if constexpr (p_lo + 2 < p_hi) { constexpr int e = p_lo + 2; if constexpr (e & 1) c.rs1[e >> 4] = fadd_s(c.rs1[e >> 4], c.S[X][0][e >> 4][e & 15]); else c.rs0[e >> 4] = fadd_s(c.rs0[e >> 4], c.S[X][0][e >> 4][e & 15]); }
    }
    if constexpr (e_lo + 0 < e_hi) c.S[X][0][(e_lo + 0) >> 4][(e_lo + 0) & 15] = __builtin_amdgcn_exp2f(c.S[X][0][(e_lo + 0) >> 4][(e_lo + 0) & 15]);
    if constexpr (e_lo + 1 < e_hi) c.S[X][0][(e_lo + 1) >> 4][(e_lo + 1) & 15] = __builtin_amdgcn_exp2f(c.S[X][0][(e_lo + 1) >> 4][(e_lo + 1) & 15]);
    if constexpr (e_lo + 2 < e_hi) c.S[X][0][(e_lo + 2) >> 4][(e_lo + 2) & 15] = __builtin_amdgcn_exp2f(c.S[X][0][(e_lo + 2) >> 4][(e_lo + 2) & 15]);
  }
  if constexpr (M == 31) asm volatile("" :: "v"(c.S[X][0][0]), "v"(c.S[X][0][1]), "v"(c.NEGM[0]), "v"(c.NEGM[1]), "v"(c.rs0[0]), "v"(c.rs0[1]), "v"(c.rs1[0]), "v"(c.rs1[1]));
  GAP();
}
template <int X, bool PEEL, bool ZERO1, bool DK, bool DV, bool KR, int... Ms> DI void phaseB_(W4C& c, const int t, iseq<Ms...>) {
  const unsigned kslot_rd = (unsigned)((t + 1) & 1) * 0x4000u;
  if constexpr (!PEEL) asm volatile("s_nop 1" ::: "memory");
  GAP();
  (gapB<X, PEEL, ZERO1, DK, DV, KR, Ms>(c, kslot_rd), ...);
  {
    #pragma unroll
    for (int e = (32 * 12) / 13; e < 32; ++e) { if (e & 1) c.rs1[e >> 4] = fadd_s(c.rs1[e >> 4], c.S[X][0][e >> 4][e & 15]); else c.rs0[e >> 4] = fadd_s(c.rs0[e >> 4], c.S[X][0][e >> 4][e & 15]); }
  }
  {
    if (__builtin_expect(c.pend, 0)) {
      asm volatile("s_nop 7\n\ts_nop 7" ::: "memory");
      acc_scale_all(c.fct[0], c.fct[1], make_iseq<128>{});
      asm volatile("s_nop 1" ::: "memory");
      c.lsum[0] = fmul_s(c.lsum[0], c.fct[0]); c.lsum[1] = fmul_s(c.lsum[1], c.fct[1]); c.pend = false;
    }
  }
  asm volatile("s_waitcnt lgkmcnt(0)" ::: "memory");
  GAP();
}
template <int X, bool PEEL, bool ZERO1, bool DK = true, bool DV = true, bool KR = true> DI void phaseB(W4C& c, const int t) { phaseB_<X, PEEL, ZERO1, DK, DV, KR>(c, t, make_iseq<32>{}); }

template <int... Is> DI void k_reads(W4C& c, unsigned slot, iseq<Is...>) { (k_read<Is>(c, slot), ...); }
template <int... Is> DI void tr_reads(W4C& c, unsigned vslot, iseq<Is...>) { (tr_read<Is>(c, vslot), ...); }
template <int X, int... Cs> DI void packs(W4C& c, iseq<Cs...>) { (pack1<X, ((Cs >> 3) & 1), 2 * (Cs >> 4) + ((Cs >> 2) & 1), (Cs & 3)>(c), ...); }
template <int X, int J> DI void rowsum1_padded(W4C& c) { rowsum1<X, J>(c); asm volatile("s_nop 1"); }
template <int X, int... Js> DI void rowsums(W4C& c, iseq<Js...>) { (rowsum1_padded<X, Js>(c), ...); }
template <int... Ms> DI void pv_drain(W4C& c, iseq<Ms...>) { ((pv_mfma<Ms, false>(c), GAP()), ...); }

template <int QB, int D0> DI void tree_qk(f32x16& S, const bf16x8& kf) {
  constexpr int aq = AQ_(QB, D0);
  if constexpr (D0 == 7) asm volatile("s_nop 1\n\tv_mfma_f32_32x32x16_bf16 %0, %1, a[%c2:%c3], %0\n\ts_nop 11" : "+v"(S) : "v"(kf), "i"(aq), "i"(aq + 3) : W4_ACLOB, W4_ACLOB_HI);
  else                   asm volatile("s_nop 1\n\tv_mfma_f32_32x32x16_bf16 %0, %1, a[%c2:%c3], %0" : "+v"(S) : "v"(kf), "i"(aq), "i"(aq + 3) : W4_ACLOB, W4_ACLOB_HI);
}
template <int QB, int DB> DI void tree_pv(const bf16x8& vf, const bf16x8& pk) {
  constexpr int ao = AO_(DB, QB);
  asm volatile("s_nop 1\n\tv_mfma_f32_32x32x16_bf16 a[%c2:%c3], %0, %1, a[%c2:%c3]" :: "v"(vf), "v"(pk), "i"(ao), "i"(ao + 15) : W4_ACLOB);
}
DI float bf16r(float x) { unsigned u = cvtpk(x, 0.f) & 0xFFFFu; return __uint_as_float(u << 16); }
DI float rec_bias(unsigned rec) {
  const unsigned mb = rec >> 16;
  if (mb == 0x3F80u) return 0.f;
  const float mv = __uint_as_float(mb << 16);
  return bf16r(bf16r(1.0f - mv) * -1.0e9f) * 1.4426950408889634f;
}
struct TreeSt {
  unsigned j, n0, ntt, cnt0, cnt1, g0;
  unsigned tkb, tvb, trb;
  __amdgpu_buffer_rsrc_t srdR, srdKt, srdVt;
};
DI unsigned bperm(unsigned srclane, unsigned v) { return (unsigned)__builtin_amdgcn_ds_bpermute((int)(srclane * 4u), (int)v); }
DI unsigned lane_id() { unsigned l; asm volatile("v_mbcnt_lo_u32_b32 %0, -1, 0\n\tv_mbcnt_hi_u32_b32 %0, -1, %0" : "=v"(l)); return l; }
DI void tree_rec_dma(const TreeSt& ts, unsigned i) {
  const unsigned l32 = lane_id() & 31u;
  const bool q1 = i >= ts.n0; const unsigned base = (q1 ? i - ts.n0 : i) * 32u + l32;
  unsigned off = ((ts.g0 + (q1 ? 1u : 0u)) * REC_CAP + base) * 4u;
  if (!(i < ts.ntt && base < (q1 ? ts.cnt1 : ts.cnt0))) off = 0xFFFFFFF0u;
  asm volatile("s_nop 4\n\ts_mov_b32 m0, %0\n\ts_nop 0\n\tbuffer_load_dword %1, %2, 0 offen lds" :: "s"(rfl(ts.trb + 256u * (i & 1u))), "v"(off), "s"(ts.srdR) : "m0", "memory");
}
DI unsigned tree_rec_read(const TreeSt& ts, unsigned i, unsigned lane) {
  unsigned r;
  asm volatile("ds_read_b32 %0, %1\n\ts_waitcnt lgkmcnt(0)" : "=v"(r) : "v"(ts.trb + 256u * (i & 1u) + 4u * (lane & 31u)) : "memory");
  return r;
}
DI void tree_issue(TreeSt& ts, unsigned i) {
  const unsigned lane = lane_id(), hi = lane >> 5;
  const unsigned rec = tree_rec_read(ts, i, lane);
  #pragma unroll
  for (int p = 0; p < 8; ++p) {
    const unsigned kk = bperm(4u * p + (lane >> 4), rec) & 0x7FFu;
    const unsigned ko = kk * 256u + 16u * ((lane & 15u) ^ ((4u * p + (lane >> 4)) & 7u));
    asm volatile("s_mov_b32 m0, %0\n\ts_nop 0\n\tbuffer_load_dwordx4 %1, %2, 0 offen lds" :: "s"(ts.tkb + 1024u * p), "v"(ko), "s"(ts.srdKt) : "m0", "memory");
  }
  #pragma unroll
  for (int g8 = 0; g8 < 4; ++g8) {
    const unsigned vk = bperm(8u * g8 + ((lane >> 2) & 7u), rec) & 0x7FFu;
    const unsigned vo = vk * 256u + 64u * hi + 16u * (lane & 3u);
    asm volatile("s_mov_b32 m0, %0\n\ts_nop 0\n\tbuffer_load_dwordx4 %1, %2, 0 offen lds" :: "s"(ts.tvb + 2048u * g8), "v"(vo), "s"(ts.srdVt) : "m0", "memory");
    asm volatile("s_mov_b32 m0, %0\n\ts_nop 0\n\tbuffer_load_dwordx4 %1, %2, %3 offen lds" :: "s"(ts.tvb + 2048u * g8 + 1024u), "v"(vo), "s"(ts.srdVt), "s"(128u) : "m0", "memory");
  }
}
template <int QB, int X, bool INLOOP> DI void tree_compute(W4C& c, const TreeSt& ts) {
  const unsigned lane = lane_id(), l32 = lane & 31u, hi = lane >> 5;
  const unsigned vkt = ts.tkb + 256u * l32;
  const unsigned vvt = ts.tvb + (((lane & 3u) << 3) | (((lane >> 2) & 3u) << 6) | (((lane >> 4) & 1u) << 5) | (hi << 8));
  f32x16 S;
  const unsigned rec_cur = tree_rec_read(ts, ts.j, lane);
  #pragma unroll
  for (int g4 = 0; g4 < 4; ++g4) {
    #pragma unroll
    for (int k = 0; k < 4; ++k) {
      const unsigned rj = bperm(8u * g4 + 4u * hi + k, rec_cur);
      const bool own = ((rj >> 11) & 15u) == (l32 & 7u) + 1u;
      S[4 * g4 + k] = own ? (rec_bias(rj) - c.mhat[QB]) : -1.0e30f;
    }
  }
  bf16x8 kf[8];
  #pragma unroll
  for (int d0 = 0; d0 < 8; ++d0) asm volatile("ds_read_b128 %0, %1 offset:%2" : "=v"(kf[d0]) : "v"(vkt + 16u * ((2u * (d0 & 3) + hi) ^ (l32 & 7u))), "i"(128 * (d0 >> 2)) : "memory");
  asm volatile("s_waitcnt lgkmcnt(0)" : "+v"(kf[0]), "+v"(kf[1]), "+v"(kf[2]), "+v"(kf[3]), "+v"(kf[4]), "+v"(kf[5]), "+v"(kf[6]), "+v"(kf[7]) :: "memory");
  tree_qk<QB, 0>(S, kf[0]); tree_qk<QB, 1>(S, kf[1]); tree_qk<QB, 2>(S, kf[2]); tree_qk<QB, 3>(S, kf[3]);
  tree_qk<QB, 4>(S, kf[4]); tree_qk<QB, 5>(S, kf[5]); tree_qk<QB, 6>(S, kf[6]); tree_qk<QB, 7>(S, kf[7]);
  float mx = max3f(S[0], S[1], S[2]);
  mx = max3f(mx, S[3], S[4]); mx = max3f(mx, S[5], S[6]); mx = max3f(mx, S[7], S[8]); mx = max3f(mx, S[9], S[10]);
  mx = max3f(mx, S[11], S[12]); mx = max3f(mx, S[13], S[14]); mx = max2f(mx, S[15]);
  { auto pr = __builtin_amdgcn_permlane32_swap(__float_as_uint(mx), __float_as_uint(mx), false, false);
    mx = max2f(__uint_as_float(pr[0]), __uint_as_float(pr[1])); }
  if (__builtin_expect(__any((int)(mx > c.THRv)), 0)) {
    const float dl = max2f(mx, 0.f), f = __builtin_amdgcn_exp2f(-dl);
    c.mhat[QB] = fadd_s(c.mhat[QB], dl);
    #pragma unroll
    for (int e = 0; e < 16; ++e) S[e] = fsub_s(S[e], dl);
    asm volatile("s_nop 7\n\ts_nop 7" ::: "memory");
    acc_scale_qb<QB>(f, make_iseq<64>{});
    asm volatile("s_nop 1" ::: "memory");
    c.lsum[QB] = fmul_s(c.lsum[QB], f);
    if constexpr (INLOOP) {
      #pragma unroll
      for (int e = 0; e < 16; ++e) { c.S[X][0][QB][e] = fmul_s(c.S[X][0][QB][e], f); c.S[X][1][QB][e] = fsub_s(c.S[X][1][QB][e], dl); }
      c.rs0[QB] = fmul_s(c.rs0[QB], f); c.rs1[QB] = fmul_s(c.rs1[QB], f);
    }
    negm_bcast(c.NEGM[QB], w4_aone(), -c.mhat[QB]);
  }
  float rs = 0.f;
  #pragma unroll
  for (int e = 0; e < 16; ++e) { S[e] = __builtin_amdgcn_exp2f(S[e]); rs += S[e]; }
  { auto pr = __builtin_amdgcn_permlane32_swap(__float_as_uint(rs), __float_as_uint(rs), false, false);
    c.lsum[QB] = c.lsum[QB] + __uint_as_float(pr[0]) + __uint_as_float(pr[1]); }
  bf16x8 pk[2];
  #pragma unroll
  for (int ks = 0; ks < 2; ++ks) { u32x4 w;
    #pragma unroll
    for (int p = 0; p < 4; ++p) w[p] = cvtpk(S[8 * ks + 2 * p], S[8 * ks + 2 * p + 1]);
    pk[ks] = __builtin_bit_cast(bf16x8, w); }
  #pragma unroll
  for (int ks = 0; ks < 2; ++ks) {
    bf16x4 lo[4], up[4];
    #pragma unroll
    for (int db = 0; db < 4; ++db) {
      asm volatile("ds_read_b64_tr_b16 %0, %1 offset:%2" : "=v"(lo[db]) : "v"(vvt), "i"(4096 * ks + 512 * db) : "memory");
      asm volatile("ds_read_b64_tr_b16 %0, %1 offset:%2" : "=v"(up[db]) : "v"(vvt), "i"(4096 * ks + 2048 + 512 * db) : "memory");
    }
    asm volatile("s_waitcnt lgkmcnt(0)" : "+v"(lo[0]), "+v"(lo[1]), "+v"(lo[2]), "+v"(lo[3]), "+v"(up[0]), "+v"(up[1]), "+v"(up[2]), "+v"(up[3]) :: "memory");
    tree_pv<QB, 0>(__builtin_shufflevector(lo[0], up[0], 0, 1, 2, 3, 4, 5, 6, 7), pk[ks]);
    tree_pv<QB, 1>(__builtin_shufflevector(lo[1], up[1], 0, 1, 2, 3, 4, 5, 6, 7), pk[ks]);
    tree_pv<QB, 2>(__builtin_shufflevector(lo[2], up[2], 0, 1, 2, 3, 4, 5, 6, 7), pk[ks]);
    tree_pv<QB, 3>(__builtin_shufflevector(lo[3], up[3], 0, 1, 2, 3, 4, 5, 6, 7), pk[ks]);
  }
  asm volatile("s_nop 1" ::: "memory");
}
template <int X, bool INLOOP> DI void tree_block(W4C& c, TreeSt& ts) {
  if (ts.j < ts.ntt) {
    if constexpr (!INLOOP) asm volatile("s_waitcnt vmcnt(0)" ::: "memory");
    if (ts.j < ts.n0) tree_compute<0, X, INLOOP>(c, ts); else tree_compute<1, X, INLOOP>(c, ts);
    ts.j += 1u;
    if (ts.j < ts.ntt) tree_issue(ts, ts.j);
    tree_rec_dma(ts, ts.j + 1u);
  }
}
template <int R> DI void acc_zero1() { asm volatile("v_accvgpr_write_b32 a[%c0], 0" :: "i"(R) : W4_ACLOB); }
template <int... Rs> DI void acc_zero(iseq<Rs...>) { (acc_zero1<Rs>(), ...); }

extern "C" __global__ __launch_bounds__(256, 1)
void tree_attn_main(const unsigned short* __restrict__ Q, const unsigned short* __restrict__ Kc, const unsigned short* __restrict__ Vc,
                    const unsigned short* __restrict__ Ktr, const unsigned short* __restrict__ Vtr,
                    const unsigned* __restrict__ cnts, const unsigned* __restrict__ recs, unsigned short* __restrict__ O) {
  __shared__ __attribute__((aligned(16))) char lds[LDS_BYTES];
  const unsigned tid = threadIdx.x, lane = tid & 63u, wid = tid >> 6, l32 = lane & 31u, hi = lane >> 5;
  const float CSC = 0.12751743082f;
  W4C c;
  c.THRv = THR;
  c.ldsb = (unsigned)(size_t)lds;
  #pragma unroll
  for (int j = 0; j < 4; ++j) c.vkb[j] = c.ldsb + KRING + 256u * l32 + 16u * ((2u * j + hi) ^ (l32 & 7u));
  c.vvb = c.ldsb + VRING + (((lane & 3u) << 3) | (((lane >> 2) & 3u) << 6) | (((lane >> 4) & 1u) << 5) | (hi << 8));
  #pragma unroll
  for (int par = 0; par < 2; ++par) c.vksrc[par] = KRS * (lane >> 4) + 16u * ((lane & 15u) ^ (4u * par + (lane >> 4)));
  c.vvsrc = KRS * ((lane >> 2) & 7u) + 64u * hi + 16u * (lane & 3u);
  c.m0k = rfl(c.ldsb + KRING + 4096u * wid); c.m0v = rfl(c.ldsb + VRING + 4096u * wid);
  c.sofk = rfl(KRS * 16u * wid); c.sofv = rfl(KRS * 16u * wid);

  const unsigned blk = blockIdx.x, kvh = blk & 7u, pblk = blk >> 3;
  const unsigned P0 = 64u * pblk + 16u * wid;
  const char* Kbase = (const char*)Kc + (size_t)kvh * C * 256;
  const char* Vbase = (const char*)Vc + (size_t)kvh * C * 256;
  c.srdK = __builtin_amdgcn_make_buffer_rsrc((void*)Kbase, (short)0, C * 256, SRD_WORD3);
  c.srdV = __builtin_amdgcn_make_buffer_rsrc((void*)Vbase, (short)0, C * 256, SRD_WORD3);
  auto dma_k = [&](int tk) {
    const unsigned slot = (unsigned)(tk & 1) * 0x4000u, skv = KTILEB * (unsigned)tk;
    #pragma unroll
    for (int q = 0; q < 4; ++q)
      asm volatile("s_nop 4\n\ts_mov_b32 m0, %0\n\ts_nop 0\n\tbuffer_load_dwordx4 %1, %2, %3 offen lds"
                   :: "s"(rfl(c.m0k + slot + 1024u * q)), "v"(c.vksrc[q & 1]), "s"(c.srdK), "s"(rfl(skv + c.sofk + KRS * 4u * q)) : "m0", "memory");
  };
  auto dma_v = [&](int tv) {
    const unsigned slot = (unsigned)(tv & 1) * 0x4000u, skv = KTILEB * (unsigned)tv;
    #pragma unroll
    for (int q = 0; q < 4; ++q)
      asm volatile("s_nop 4\n\ts_mov_b32 m0, %0\n\ts_nop 0\n\tbuffer_load_dwordx4 %1, %2, %3 offen lds"
                   :: "s"(rfl(c.m0v + slot + 1024u * q)), "v"(c.vvsrc), "s"(c.srdV), "s"(rfl(skv + c.sofv + KRS * 8u * (q >> 1) + 128u * (q & 1))) : "m0", "memory");
  };
  TreeSt ts;
  ts.g0 = P0 >> 3;
  ts.srdR = __builtin_amdgcn_make_buffer_rsrc((void*)recs, (short)0, 256u * REC_CAP * 4u, SRD_WORD3);
  ts.cnt0 = cnts[ts.g0]; ts.cnt1 = cnts[ts.g0 + 1u];
  ts.n0 = (ts.cnt0 + 31u) >> 5; ts.ntt = ts.n0 + ((ts.cnt1 + 31u) >> 5); ts.j = 0u;
  ts.srdKt = __builtin_amdgcn_make_buffer_rsrc((void*)((const char*)Ktr + (size_t)kvh * T * 256), (short)0, T * 256, SRD_WORD3);
  ts.srdVt = __builtin_amdgcn_make_buffer_rsrc((void*)((const char*)Vtr + (size_t)kvh * T * 256), (short)0, T * 256, SRD_WORD3);
  ts.tkb = rfl(c.ldsb + TREEV + 16384u * wid); ts.tvb = ts.tkb + 8192u;
  ts.trb = rfl(c.ldsb + TREER + 512u * wid);
  tree_rec_dma(ts, 0u); tree_rec_dma(ts, 1u);
  bf16x8 qf[2][8];
  #pragma unroll
  for (int qb = 0; qb < 2; ++qb) {
    const char* qrow = (const char*)Q + ((size_t)(4u * kvh + (l32 >> 3)) * T + P0 + 8u * qb + (l32 & 7u)) * 256u + hi * 16u;
    #pragma unroll
    for (int d0 = 0; d0 < 8; ++d0) qf[qb][d0] = *(const bf16x8*)(qrow + 32 * d0);
  }
  dma_k(0); dma_v(0); dma_k(1);
  asm volatile("s_waitcnt vmcnt(28)" ::: "memory");
  if (0u < ts.ntt) tree_issue(ts, 0u);
  acc_zero(make_iseq<128>{});
  {
    u32x4 w;
    #define QW(I) { const bf16x8 q8 = qf[(I) >> 3][(I) & 7]; _Pragma("unroll") for (int e = 0; e < 4; ++e) w[e] = cvtpk(fmul_s((float)q8[2 * e], CSC), fmul_s((float)q8[2 * e + 1], CSC)); q_write1<I>(w); }
    QW(0) QW(1) QW(2) QW(3) QW(4) QW(5) QW(6) QW(7) QW(8) QW(9) QW(10) QW(11) QW(12) QW(13) QW(14) QW(15)
    #undef QW
  }
  c.mhat[0] = c.mhat[1] = 0.f; c.lsum[0] = c.lsum[1] = 0.f; c.fct[0] = c.fct[1] = 1.f; c.pend = false;
  asm volatile("s_waitcnt vmcnt(0)\n\ts_barrier" ::: "memory");
  k_reads(c, 0u, make_iseq<16>{});
  asm volatile("s_waitcnt lgkmcnt(0)\n\ts_nop 1" ::: "memory");

  phaseA<0, true>(c, 0);  phaseB<0, true,  false>(c, 0);
  phaseA<1, false>(c, 1); phaseB<1, false, false>(c, 1); tree_block<1, true>(c, ts);
  for (int t = 2; t < NT - 2; t += 2) {
    phaseA<0, false>(c, t);     phaseB<0, false, false>(c, t);     tree_block<0, true>(c, ts);
    phaseA<1, false>(c, t + 1); phaseB<1, false, false>(c, t + 1); tree_block<1, true>(c, ts);
  }
  phaseA<0, false>(c, NT - 2); phaseB<0, false, false,  false,  true,  true>(c, NT - 2);
  phaseA<1, false>(c, NT - 1); phaseB<1, false, false,  false,  false,  false>(c, NT - 1);
  {
    const unsigned vslot = (unsigned)((NT - 1) & 1) * 0x4000u;
    tr_reads(c, vslot, make_iseq<32>{});
    #pragma unroll
    for (int e = 0; e < 32; ++e) c.S[1][1][e >> 4][e & 15] = __builtin_amdgcn_exp2f(c.S[1][1][e >> 4][e & 15]);
    packs<1>(c, make_iseq<32>{});
    rowsums<1>(c, iseq<8, 9, 10, 11, 12, 13, 14, 15, 24, 25, 26, 27, 28, 29, 30, 31>{});
    #pragma unroll
    for (int qb = 0; qb < 2; ++qb) { const float rs = fadd_s(c.rs0[qb], c.rs1[qb]);
      auto pr = __builtin_amdgcn_permlane32_swap(__float_as_uint(rs), __float_as_uint(rs), false, false);
      c.lsum[qb] = fadd_s(c.lsum[qb], fadd_s(__uint_as_float(pr[0]), __uint_as_float(pr[1]))); }
    asm volatile("s_waitcnt lgkmcnt(0)" : W4_VLO :: "memory"); asm volatile("" : W4_VUP :: "memory");
    GAP();
    #pragma unroll
    for (int ks = 0; ks < 4; ++ks)
      #pragma unroll
      for (int db = 0; db < 4; ++db) c.Vf[db][ks] = __builtin_shufflevector(c.vlo[db][ks], c.vup[db][ks], 0, 1, 2, 3, 4, 5, 6, 7);
    asm volatile("s_nop 1" ::: "memory");
    pv_drain(c, make_iseq<32>{});
    asm volatile("s_nop 7\n\ts_nop 7" ::: "memory");
  }
  while (ts.j < ts.ntt) tree_block<0, false>(c, ts);
  asm volatile("s_barrier" ::: "memory");
  {
    char* stg = lds + 16384u * wid;
    #pragma unroll
    for (int qb = 0; qb < 2; ++qb) {
      const float il = c.lsum[qb] > 0.f ? __builtin_amdgcn_rcpf(c.lsum[qb]) : 0.f;
      #define EPI_DB(DB) { float o[16]; if (qb == 0) acc_read16<DB, 0>(o, make_iseq<16>{}); else acc_read16<DB, 1>(o, make_iseq<16>{}); \
        _Pragma("unroll") for (int g = 0; g < 4; ++g) { u32x2 w; w[0] = cvtpk(o[4 * g] * il, o[4 * g + 1] * il); w[1] = cvtpk(o[4 * g + 2] * il, o[4 * g + 3] * il); \
          *(u32x2*)(stg + 32u * 256u * qb + l32 * 256u + 16u * ((4u * DB + g) ^ (l32 & 15u)) + 8u * hi) = w; } }
      EPI_DB(0) EPI_DB(1) EPI_DB(2) EPI_DB(3)
      #undef EPI_DB
    }
    asm volatile("s_waitcnt lgkmcnt(0)" ::: "memory");
    #pragma unroll
    for (int j = 0; j < 16; ++j) {
      const unsigned r = 4u * j + (lane >> 4), cc = lane & 15u;
      const u32x4 v = *(const u32x4*)(stg + r * 256u + 16u * (cc ^ (r & 15u)));
      char* orow = (char*)O + ((size_t)(4u * kvh + ((r >> 3) & 3u)) * T + P0 + 8u * (r >> 5) + (r & 7u)) * 256u + 16u * cc;
      *(u32x4*)orow = v;
    }
  }
}

__global__ __launch_bounds__(256) void tree_prepass(const u32x4* __restrict__ mask, unsigned* __restrict__ cnts, unsigned* __restrict__ recs) {
  __shared__ unsigned wcnt[4];
  const unsigned g = blockIdx.x, tid = threadIdx.x, lane = tid & 63u, wid = tid >> 6;
  u32x4 v[8];
  #pragma unroll
  for (int r = 0; r < 8; ++r) v[r] = mask[(size_t)(8u * g + r) * 256u + tid];
  unsigned nz = 0;
  #pragma unroll
  for (int r = 0; r < 8; ++r)
    #pragma unroll
    for (int k = 0; k < 4; ++k) nz += ((v[r][k] & 0x7FFFu) != 0u) + ((v[r][k] & 0x7FFF0000u) != 0u);
  unsigned tot = 0;
  if (__any(nz != 0u)) {
    #pragma unroll
    for (int o = 32; o >= 1; o >>= 1) nz += __shfl_xor(nz, o);
    tot = nz;
  }
  if (lane == 0) wcnt[wid] = tot;
  __syncthreads();
  unsigned base = 0;
  for (unsigned w = 0; w < wid; ++w) base += wcnt[w];
  if (tid == 0) cnts[g] = wcnt[0] + wcnt[1] + wcnt[2] + wcnt[3];
  if (tot == 0) return;
  unsigned* rg = recs + (size_t)g * REC_CAP;
  #pragma unroll
  for (int r = 0; r < 8; ++r) {
    if (!__any((v[r][0] | v[r][1] | v[r][2] | v[r][3]) & 0x7FFF7FFFu)) continue;
    #pragma unroll
    for (int e = 0; e < 8; ++e) {
      const unsigned bits = (v[r][e >> 1] >> (16 * (e & 1))) & 0xFFFFu;
      const bool on = (bits & 0x7FFFu) != 0u;
      const unsigned long long m = __ballot(on);
      if (on) rg[base + __popcll(m & ((1ull << lane) - 1ull))] = (8u * tid + e) | ((unsigned)(r + 1) << 11) | (bits << 16);
      base += __popcll(m);
    }
  }
}

extern "C" void kernel_launch(void* const* d_in, const int* in_sizes, int n_in, void* d_out, int out_size, void* d_ws, size_t ws_size, hipStream_t stream) {
  unsigned* cnts = (unsigned*)d_ws;
  unsigned* recs = (unsigned*)((char*)d_ws + 4096);
  hipLaunchKernelGGL(tree_prepass, dim3(256), dim3(256), 0, stream, (const u32x4*)d_in[5], cnts, recs);
  hipLaunchKernelGGL(tree_attn_main, dim3(256), dim3(256), 0, stream, (const unsigned short*)d_in[0], (const unsigned short*)d_in[1], (const unsigned short*)d_in[2],
                     (const unsigned short*)d_in[3], (const unsigned short*)d_in[4], (const unsigned*)cnts, (const unsigned*)recs, (unsigned short*)d_out);
}
```

```cpp
#include <hip/hip_runtime.h>
#include <hip/hip_bf16.h>
#include <stdint.h>
#include <utility>

constexpr int D = 128, HQ = 32, HKV = 8, T = 2048, C = 2048, NT = C / 64;
constexpr float THR = 8.0f;
constexpr unsigned KRS = 256u, KTILEB = 64u * KRS;
constexpr unsigned REC_CAP = 8u * 2048u;

typedef float    f32x16 __attribute__((ext_vector_type(16)));
typedef float    f32x2  __attribute__((ext_vector_type(2)));
typedef float    f32x4  __attribute__((ext_vector_type(4)));
typedef __bf16   bf16x8 __attribute__((ext_vector_type(8)));
typedef __bf16   bf16x4 __attribute__((ext_vector_type(4)));
typedef __bf16   bf16x2 __attribute__((ext_vector_type(2)));
typedef unsigned u32x4  __attribute__((ext_vector_type(4)));
typedef unsigned u32x2  __attribute__((ext_vector_type(2)));
template <int... Is> using iseq = std::integer_sequence<int, Is...>;
template <int N> using make_iseq = std::make_integer_sequence<int, N>;

#define KRING 0x0000u
#define VRING 0x8000u
#define TREEV 0x10000u
#define TREER 0x20000u
#define LDS_BYTES 0x20800u
#define SRD_WORD3 0x00020000
#define DI __device__ __forceinline__
#define GAP() __builtin_amdgcn_sched_barrier(0)
#define W4_ACLOB_HI "a128","a129","a130","a131","a132","a133","a134","a135","a136","a137","a138","a139","a140","a141","a142","a143","a144","a145","a146","a147","a148","a149","a150","a151","a152","a153","a154","a155","a156","a157","a158","a159","a160","a161","a162","a163","a164","a165","a166","a167","a168","a169","a170","a171","a172","a173","a174","a175","a176","a177","a178","a179","a180","a181","a182","a183","a184","a185","a186","a187","a188","a189","a190","a191","a192","a193","a194","a195","a196","a197","a198","a199","a200","a201","a202","a203","a204","a205","a206","a207","a208","a209","a210","a211","a212","a213","a214","a215","a216","a217","a218","a219","a220","a221","a222","a223","a224","a225","a226","a227","a228","a229","a230","a231","a232","a233","a234","a235","a236","a237","a238","a239","a240","a241","a242","a243","a244","a245","a246","a247","a248","a249","a250","a251","a252","a253","a254","a255"
#define W4_ACLOB "a0","a1","a2","a3","a4","a5","a6","a7","a8","a9","a10","a11","a12","a13","a14","a15","a16","a17","a18","a19","a20","a21","a22","a23","a24","a25","a26","a27","a28","a29","a30","a31","a32","a33","a34","a35","a36","a37","a38","a39","a40","a41","a42","a43","a44","a45","a46","a47","a48","a49","a50","a51","a52","a53","a54","a55","a56","a57","a58","a59","a60","a61","a62","a63","a64","a65","a66","a67","a68","a69","a70","a71","a72","a73","a74","a75","a76","a77","a78","a79","a80","a81","a82","a83","a84","a85","a86","a87","a88","a89","a90","a91","a92","a93","a94","a95","a96","a97","a98","a99","a100","a101","a102","a103","a104","a105","a106","a107","a108","a109","a110","a111","a112","a113","a114","a115","a116","a117","a118","a119","a120","a121","a122","a123","a124","a125","a126","a127"

DI unsigned rfl(unsigned x) { return __builtin_amdgcn_readfirstlane(x); }
DI unsigned cvtpk(float lo, float hi) {
  f32x2 v = {lo, hi}; bf16x2 b = __builtin_convertvector(v, bf16x2); return __builtin_bit_cast(unsigned, b);
}
DI float max3f(float a, float b, float c) { float r; asm("v_max3_f32 %0, %1, %2, %3" : "=v"(r) : "v"(a), "v"(b), "v"(c)); return r; }
DI float max2f(float a, float b) { float r; asm("v_max_f32_e32 %0, %1, %2" : "=v"(r) : "v"(a), "v"(b)); return r; }
DI float fadd_s(float a, float b) { float r; asm("v_add_f32_e32 %0, %1, %2" : "=v"(r) : "v"(a), "v"(b)); return r; }
DI float fmul_s(float a, float b) { float r; asm("v_mul_f32_e32 %0, %1, %2" : "=v"(r) : "v"(a), "v"(b)); return r; }
DI float fsub_s(float a, float b) { float r; asm("v_sub_f32_e32 %0, %1, %2" : "=v"(r) : "v"(a), "v"(b)); return r; }
DI float w4_aone() { unsigned l; asm volatile("v_mbcnt_lo_u32_b32 %0, -1, 0\n\tv_mbcnt_hi_u32_b32 %0, -1, %0" : "=v"(l)); return l < 32u ? 1.f : 0.f; }
DI void negm_bcast(f32x16& negm, float aone, float neg_mhat) {
  asm volatile("s_nop 1\n\tv_mfma_f32_32x32x2_f32 %0, %1, %2, 0" : "+v"(negm) : "v"(aone), "v"(neg_mhat));
}

struct W4C {
  unsigned vkb[4], vvb, vksrc[2], vvsrc, m0k, m0v, sofk, sofv, ldsb;
  __amdgpu_buffer_rsrc_t srdK, srdV;
  float THRv;
  f32x16 S[2][2][2];
  f32x16 NEGM[2];
  bf16x8 Vf[4][4];
  bf16x8 Pk[2][4];
  bf16x4 vlo[4][4], vup[4][4];
  float mhat[2], lsum[2], fct[2]; bool pend;
  float rs0[2], rs1[2], pmx0[2], pmx1[2], rmax[2];
  unsigned pm0[8], psf[8];
};

constexpr int AO_(int db, int qb) { return 16 * (2 * db + qb); }
constexpr int AQ_(int qb, int d0) { return 128 + 32 * qb + 4 * d0; }
constexpr int AK_(int kb, int d0) { return 192 + 32 * kb + 4 * d0; }

template <int I> DI void tr_read(W4C& c, unsigned vslot) {
  constexpr int ks = I >> 3, db = (I >> 1) & 3;
  if constexpr ((I & 1) == 0) asm volatile("ds_read_b64_tr_b16 %0, %1 offset:%2" : "=v"(c.vlo[db][ks]) : "v"(c.vvb + vslot), "i"(4096 * ks + 512 * db) : "memory");
  else                        asm volatile("ds_read_b64_tr_b16 %0, %1 offset:%2" : "=v"(c.vup[db][ks]) : "v"(c.vvb + vslot), "i"(4096 * ks + 2048 + 512 * db) : "memory");
}
template <int I> DI void k_read(W4C& c, unsigned slot) {
  constexpr int kb = I >> 3, d0 = I & 7;
  asm volatile("ds_read_b128 a[%c1:%c2], %0 offset:%c3" :: "v"(c.vkb[d0 & 3] + slot), "i"(AK_(kb, d0)), "i"(AK_(kb, d0) + 3), "i"(8192 * kb + 128 * (d0 >> 2)) : "memory", W4_ACLOB_HI);
}
template <int I> DI void q_write1(const u32x4& w) {
  constexpr int aq = AQ_(I >> 3, I & 7);
  asm volatile("v_accvgpr_write_b32 a[%c4], %0\n\tv_accvgpr_write_b32 a[%c5], %1\n\tv_accvgpr_write_b32 a[%c6], %2\n\tv_accvgpr_write_b32 a[%c7], %3"
               :: "v"(w[0]), "v"(w[1]), "v"(w[2]), "v"(w[3]), "i"(aq), "i"(aq + 1), "i"(aq + 2), "i"(aq + 3) : W4_ACLOB_HI);
}
template <int X, bool FIRST, int M> DI void qk_mfma(W4C& c) {
  constexpr int d0 = M >> 2, kb = (M >> 1) & 1, qb = M & 1, ak = AK_(kb, d0), aq = AQ_(qb, d0);
  if constexpr (d0 == 0) {
    if constexpr (FIRST) asm volatile("v_mfma_f32_32x32x16_bf16 %0, a[%c1:%c2], a[%c3:%c4], 0" : "=&v"(c.S[X][kb][qb]) : "i"(ak), "i"(ak + 3), "i"(aq), "i"(aq + 3) : W4_ACLOB, W4_ACLOB_HI);
    else asm volatile("v_mfma_f32_32x32x16_bf16 %0, a[%c2:%c3], a[%c4:%c5], %1" : "=&v"(c.S[X][kb][qb]) : "v"(c.NEGM[qb]), "i"(ak), "i"(ak + 3), "i"(aq), "i"(aq + 3) : W4_ACLOB, W4_ACLOB_HI);
  } else asm volatile("v_mfma_f32_32x32x16_bf16 %0, a[%c1:%c2], a[%c3:%c4], %0" : "+v"(c.S[X][kb][qb]) : "i"(ak), "i"(ak + 3), "i"(aq), "i"(aq + 3) : W4_ACLOB, W4_ACLOB_HI);
}
template <int M, bool ZERO> DI void pv_mfma(W4C& c) {
  constexpr int ks = M >> 3, db = (M >> 1) & 3, qb = M & 1, ao = AO_(db, qb);
  if constexpr (ZERO && ks == 0) asm volatile("v_mfma_f32_32x32x16_bf16 a[%c2:%c3], %0, %1, 0" :: "v"(c.Vf[db][ks]), "v"(c.Pk[qb][ks]), "i"(ao), "i"(ao + 15) : W4_ACLOB);
  else                         asm volatile("v_mfma_f32_32x32x16_bf16 a[%c2:%c3], %0, %1, a[%c2:%c3]" :: "v"(c.Vf[db][ks]), "v"(c.Pk[qb][ks]), "i"(ao), "i"(ao + 15) : W4_ACLOB);
}
template <int X, int QB, int KS, int P> DI void pack1(W4C& c) {
  u32x4 w = __builtin_bit_cast(u32x4, c.Pk[QB][KS]);
  w[P] = cvtpk(c.S[X][KS >> 1][QB][8 * (KS & 1) + 2 * P], c.S[X][KS >> 1][QB][8 * (KS & 1) + 2 * P + 1]);
  c.Pk[QB][KS] = __builtin_bit_cast(bf16x8, w);
}
template <int X, int J> DI void rowsum1(W4C& c) {
  constexpr int qb = J >> 4, ks = (J >> 2) & 3, p = J & 3;
  c.rs0[qb] = fadd_s(c.rs0[qb], c.S[X][ks >> 1][qb][8 * (ks & 1) + 2 * p]); c.rs1[qb] = fadd_s(c.rs1[qb], c.S[X][ks >> 1][qb][8 * (ks & 1) + 2 * p + 1]);
}

template <int R> DI void acc_scale1(float f) {
  float t;
  asm volatile("v_accvgpr_read_b32 %0, a[%c1]\n\tv_mul_f32 %0, %2, %0\n\tv_accvgpr_write_b32 a[%c1], %0" : "=&v"(t) : "i"(R), "v"(f) : W4_ACLOB);
}
template <int... Rs> DI void acc_scale_all(float f0, float f1, iseq<Rs...>) { (acc_scale1<Rs>(((Rs >> 4) & 1) ? f1 : f0), ...); }
template <int QB, int... Rs> DI void acc_scale_qb(float f, iseq<Rs...>) { (acc_scale1<16 * (2 * (Rs >> 4) + QB) + (Rs & 15)>(f), ...); }
template <int R> DI float acc_read1() { float t; asm volatile("v_accvgpr_read_b32 %0, a[%c1]" : "=v"(t) : "i"(R)); return t; }
template <int DB, int QB, int... Rs> DI void acc_read16(float (&o)[16], iseq<Rs...>) { ((o[Rs] = acc_read1<AO_(DB, QB) + Rs>()), ...); }

template <int X, bool FIRST, int M> DI void gapA(W4C& c, unsigned vslot, const int t) {
  constexpr int Xp = X ^ 1;
  qk_mfma<X, FIRST, M>(c);
  if constexpr (M >= 16 && M <= 31) {
    const int tk = t + 2, tv = t + 1;
    constexpr int q = (M - 16) >> 1;
    if constexpr (((M - 16) & 1) == 0) {
      if constexpr (q < 4) c.pm0[q] = c.m0k + (unsigned)((t + 2) & 1) * 0x4000u + 1024u * q; else c.pm0[q] = c.m0v + (unsigned)((t + 1) & 1) * 0x4000u + 1024u * (q - 4);
      asm volatile("" : "+s"(c.pm0[q]));
    } else {
      if constexpr (q < 4) c.psf[q] = KTILEB * (unsigned)tk + c.sofk + KRS * 4u * q; else c.psf[q] = KTILEB * (unsigned)tv + c.sofv + KRS * 8u * ((q - 4) >> 1) + 128u * ((q - 4) & 1);
      asm volatile("" : "+s"(c.psf[q]));
    }
  }
  if constexpr (M == 31) asm volatile("s_nop 4" : "+s"(c.pm0[0]), "+s"(c.pm0[1]), "+s"(c.pm0[2]), "+s"(c.pm0[3]), "+s"(c.pm0[4]), "+s"(c.pm0[5]), "+s"(c.pm0[6]), "+s"(c.pm0[7]),
                                                  "+s"(c.psf[0]), "+s"(c.psf[1]), "+s"(c.psf[2]), "+s"(c.psf[3]), "+s"(c.psf[4]), "+s"(c.psf[5]), "+s"(c.psf[6]), "+s"(c.psf[7]));
  if constexpr (!FIRST && M == 8) asm volatile("" :: "v"(c.NEGM[0]), "v"(c.NEGM[1]));
  if constexpr (!FIRST) {
    constexpr int base = M + (M >= 4) + (M >= 8) + (M >= 12) + (M >= 16) + (M >= 20) + (M >= 24);
    if constexpr (M <= 25) tr_read<base>(c, vslot);
    if constexpr (M == 3 || M == 7 || M == 11 || M == 15 || M == 19 || M == 23) tr_read<base + 1>(c, vslot);
    if constexpr (M < 16) {
      constexpr int e0 = 2 * M, e1 = 2 * M + 1;
      c.S[Xp][1][e0 >> 4][e0 & 15] = __builtin_amdgcn_exp2f(c.S[Xp][1][e0 >> 4][e0 & 15]);
      c.S[Xp][1][e1 >> 4][e1 & 15] = __builtin_amdgcn_exp2f(c.S[Xp][1][e1 >> 4][e1 & 15]);
      pack1<Xp, (M >> 3), ((M >> 2) & 1), (M & 3)>(c);
    } else {
      constexpr int cc = M - 16;
      pack1<Xp, (cc >> 3), 2 + ((cc >> 2) & 1), (cc & 3)>(c);
      rowsum1<Xp, 16 * (cc >> 3) + 8 + 4 * ((cc >> 2) & 1) + (cc & 3)>(c);
    }
  }
  GAP();
}
#define W4_VLO "+v"(c.vlo[0][0]), "+v"(c.vlo[0][1]), "+v"(c.vlo[0][2]), "+v"(c.vlo[0][3]), "+v"(c.vlo[1][0]), "+v"(c.vlo[1][1]), "+v"(c.vlo[1][2]), "+v"(c.vlo[1][3]), \
               "+v"(c.vlo[2][0]), "+v"(c.vlo[2][1]), "+v"(c.vlo[2][2]), "+v"(c.vlo[2][3]), "+v"(c.vlo[3][0]), "+v"(c.vlo[3][1]), "+v"(c.vlo[3][2]), "+v"(c.vlo[3][3])
#define W4_VUP "+v"(c.vup[0][0]), "+v"(c.vup[0][1]), "+v"(c.vup[0][2]), "+v"(c.vup[0][3]), "+v"(c.vup[1][0]), "+v"(c.vup[1][1]), "+v"(c.vup[1][2]), "+v"(c.vup[1][3]), \
               "+v"(c.vup[2][0]), "+v"(c.vup[2][1]), "+v"(c.vup[2][2]), "+v"(c.vup[2][3]), "+v"(c.vup[3][0]), "+v"(c.vup[3][1]), "+v"(c.vup[3][2]), "+v"(c.vup[3][3])
template <int X, bool FIRST, int... Ms> DI void phaseA_(W4C& c, const int t, iseq<Ms...>) {
  const unsigned vslot = (unsigned)((t - 1) & 1) * 0x4000u;
  (gapA<X, FIRST, Ms>(c, vslot, t), ...);
  if constexpr (!FIRST) {
    #pragma unroll
    for (int qb = 0; qb < 2; ++qb) {
      const float rs = fadd_s(c.rs0[qb], c.rs1[qb]);
      auto pr = __builtin_amdgcn_permlane32_swap(__float_as_uint(rs), __float_as_uint(rs), false, false);
      c.lsum[qb] = fadd_s(c.lsum[qb], fadd_s(__uint_as_float(pr[0]), __uint_as_float(pr[1])));
    }
  }
  if constexpr (FIRST) asm volatile("s_waitcnt vmcnt(0) lgkmcnt(0)\n\ts_barrier" ::: "memory");
  else { asm volatile("s_waitcnt vmcnt(0) lgkmcnt(0)" : W4_VLO :: "memory"); asm volatile("s_barrier" : W4_VUP :: "memory"); }
  GAP();
  if constexpr (!FIRST) {
    #pragma unroll
    for (int ks = 0; ks < 4; ++ks)
      #pragma unroll
      for (int db = 0; db < 4; ++db) c.Vf[db][ks] = __builtin_shufflevector(c.vlo[db][ks], c.vup[db][ks], 0, 1, 2, 3, 4, 5, 6, 7);
  }
}
template <int X, bool FIRST> DI void phaseA(W4C& c, const int t) { phaseA_<X, FIRST>(c, t, make_iseq<32>{}); }

template <int X, bool PEEL, bool ZERO1, bool DK, bool DV, bool KR, int M> DI void gapB(W4C& c, const unsigned kslot_rd) {
  if constexpr (!PEEL) pv_mfma<M, ZERO1>(c);
  if constexpr (M < 4 && DK)             asm volatile("s_mov_b32 m0, %0\n\ts_nop 0\n\tbuffer_load_dwordx4 %1, %2, %3 offen lds" :: "s"(c.pm0[M]), "v"(c.vksrc[M & 1]), "s"(c.srdK),  "s"(c.psf[M]) : "m0", "memory");
  else if constexpr (M >= 4 && M < 8 && DV) asm volatile("s_mov_b32 m0, %0\n\ts_nop 0\n\tbuffer_load_dwordx4 %1, %2, %3 offen lds" :: "s"(c.pm0[M]), "v"(c.vvsrc), "s"(c.srdV),  "s"(c.psf[M]) : "m0", "memory");
  if constexpr (M >= 1 && M <= 16 && KR) k_read<M - 1>(c, kslot_rd);
  if constexpr (M >= 8 && M < 16) {
    constexpr int i = M - 8, q = i >> 2, r = 4 * (i & 3);
    if constexpr ((i & 3) == 0) { c.pmx0[q] = max3f(c.S[X][0][q][0], c.S[X][0][q][1], c.S[X][1][q][0]); c.pmx1[q] = max3f(c.S[X][0][q][2], c.S[X][0][q][3], c.S[X][1][q][1]);
                                  c.pmx0[q] = max3f(c.pmx0[q], c.S[X][1][q][2], c.S[X][1][q][3]); }
    else                        { c.pmx0[q] = max3f(c.pmx0[q], c.S[X][0][q][r], c.S[X][0][q][r + 1]); c.pmx1[q] = max3f(c.pmx1[q], c.S[X][0][q][r + 2], c.S[X][0][q][r + 3]);
                                  c.pmx0[q] = max3f(c.pmx0[q], c.S[X][1][q][r], c.S[X][1][q][r + 1]); c.pmx1[q] = max3f(c.pmx1[q], c.S[X][1][q][r + 2], c.S[X][1][q][r + 3]); }
  }
  if constexpr (M == 16 || M == 17) {
    constexpr int q = M - 16; const float mm = max2f(c.pmx0[q], c.pmx1[q]);
    auto pr = __builtin_amdgcn_permlane32_swap(__float_as_uint(mm), __float_as_uint(mm), false, false);
    c.rmax[q] = max2f(__uint_as_float(pr[0]), __uint_as_float(pr[1]));
  }
  if constexpr (M == 18) {
    if constexpr (PEEL) {
      #pragma unroll
      for (int q = 0; q < 2; ++q) { c.mhat[q] = c.rmax[q]; c.lsum[q] = 0.f; c.fct[q] = 1.f;
        #pragma unroll
        for (int kb = 0; kb < 2; ++kb) { _Pragma("unroll") for (int e = 0; e < 16; ++e) c.S[X][kb][q][e] = fsub_s(c.S[X][kb][q][e], c.rmax[q]); }
        negm_bcast(c.NEGM[q], w4_aone(), -c.mhat[q]);
      }
    } else {
      const bool grew = (c.rmax[0] > c.THRv) | (c.rmax[1] > c.THRv);
      if (__builtin_expect(__any((int)grew), 0)) {
        #pragma unroll
        for (int q = 0; q < 2; ++q) { const float dl = max2f(c.rmax[q], 0.f); c.mhat[q] = fadd_s(c.mhat[q], dl); c.fct[q] = __builtin_amdgcn_exp2f(-dl);
          #pragma unroll
          for (int kb = 0; kb < 2; ++kb) { _Pragma("unroll") for (int e = 0; e < 16; ++e) c.S[X][kb][q][e] = fsub_s(c.S[X][kb][q][e], dl); }
          negm_bcast(c.NEGM[q], w4_aone(), -c.mhat[q]);
        }
        c.pend = true;
      }
    }
  }
  if constexpr (M == 18) { c.rs0[0] = c.rs0[1] = c.rs1[0] = c.rs1[1] = 0.f; }
  if constexpr (M >= 19) {
    constexpr int g = M - 19, e_lo = (32 * g) / 13, e_hi = (32 * (g + 1)) / 13;
    {
      constexpr int p_lo = g ? (32 * (g - 1)) / 13 : 0, p_hi = g ? (32 * g) / 13 : 0;
      if constexpr (p_lo + 0 < p_hi) { constexpr int e = p_lo + 0; if constexpr (e & 1) c.rs1[e >> 4] = fadd_s(c.rs1[e >> 4], c.S[X][0][e >> 4][e & 15]); else c.rs0[e >> 4] = fadd_s(c.rs0[e >> 4], c.S[X][0][e >> 4][e & 15]); }
      if constexpr (p_lo + 1 < p_hi) { constexpr int e = p_lo + 1; if constexpr (e & 1) c.rs1[e >> 4] = fadd_s(c.rs1[e >> 4], c.S[X][0][e >> 4][e & 15]); else c.rs0[e >> 4] = fadd_s(c.rs0[e >> 4], c.S[X][0][e >> 4][e & 15]); }
      if constexpr (p_lo + 2 < p_hi) { constexpr int e = p_lo + 2; if constexpr (e & 1) c.rs1[e >> 4] = fadd_s(c.rs1[e >> 4], c.S[X][0][e >> 4][e & 15]); else c.rs0[e >> 4] = fadd_s(c.rs0[e >> 4], c.S[X][0][e >> 4][e & 15]); }
    }
    if constexpr (e_lo + 0 < e_hi) c.S[X][0][(e_lo + 0) >> 4][(e_lo + 0) & 15] = __builtin_amdgcn_exp2f(c.S[X][0][(e_lo + 0) >> 4][(e_lo + 0) & 15]);
    if constexpr (e_lo + 1 < e_hi) c.S[X][0][(e_lo + 1) >> 4][(e_lo + 1) & 15] = __builtin_amdgcn_exp2f(c.S[X][0][(e_lo + 1) >> 4][(e_lo + 1) & 15]);
    if constexpr (e_lo + 2 < e_hi) c.S[X][0][(e_lo + 2) >> 4][(e_lo + 2) & 15] = __builtin_amdgcn_exp2f(c.S[X][0][(e_lo + 2) >> 4][(e_lo + 2) & 15]);
  }
  if constexpr (M == 31) asm volatile("" :: "v"(c.S[X][0][0]), "v"(c.S[X][0][1]), "v"(c.NEGM[0]), "v"(c.NEGM[1]), "v"(c.rs0[0]), "v"(c.rs0[1]), "v"(c.rs1[0]), "v"(c.rs1[1]));
  GAP();
}
template <int X, bool PEEL, bool ZERO1, bool DK, bool DV, bool KR, int... Ms> DI void phaseB_(W4C& c, const int t, iseq<Ms...>) {
  const unsigned kslot_rd = (unsigned)((t + 1) & 1) * 0x4000u;
  if constexpr (!PEEL) asm volatile("s_nop 1" ::: "memory");
  GAP();
  (gapB<X, PEEL, ZERO1, DK, DV, KR, Ms>(c, kslot_rd), ...);
  {
    #pragma unroll
    for (int e = (32 * 12) / 13; e < 32; ++e) { if (e & 1) c.rs1[e >> 4] = fadd_s(c.rs1[e >> 4], c.S[X][0][e >> 4][e & 15]); else c.rs0[e >> 4] = fadd_s(c.rs0[e >> 4], c.S[X][0][e >> 4][e & 15]); }
  }
  {
    if (__builtin_expect(c.pend, 0)) {
      asm volatile("s_nop 7\n\ts_nop 7" ::: "memory");
      acc_scale_all(c.fct[0], c.fct[1], make_iseq<128>{});
      asm volatile("s_nop 1" ::: "memory");
      c.lsum[0] = fmul_s(c.lsum[0], c.fct[0]); c.lsum[1] = fmul_s(c.lsum[1], c.fct[1]); c.pend = false;
    }
  }
  asm volatile("s_waitcnt lgkmcnt(0)" ::: "memory");
  GAP();
}
template <int X, bool PEEL, bool ZERO1, bool DK = true, bool DV = true, bool KR = true> DI void phaseB(W4C& c, const int t) { phaseB_<X, PEEL, ZERO1, DK, DV, KR>(c, t, make_iseq<32>{}); }

template <int... Is> DI void k_reads(W4C& c, unsigned slot, iseq<Is...>) { (k_read<Is>(c, slot), ...); }
template <int... Is> DI void tr_reads(W4C& c, unsigned vslot, iseq<Is...>) { (tr_read<Is>(c, vslot), ...); }
template <int X, int... Cs> DI void packs(W4C& c, iseq<Cs...>) { (pack1<X, ((Cs >> 3) & 1), 2 * (Cs >> 4) + ((Cs >> 2) & 1), (Cs & 3)>(c), ...); }
template <int X, int J> DI void rowsum1_padded(W4C& c) { rowsum1<X, J>(c); asm volatile("s_nop 1"); }
template <int X, int... Js> DI void rowsums(W4C& c, iseq<Js...>) { (rowsum1_padded<X, Js>(c), ...); }
template <int... Ms> DI void pv_drain(W4C& c, iseq<Ms...>) { ((pv_mfma<Ms, false>(c), GAP()), ...); }

template <int QB, int D0> DI void tree_qk(f32x16& S, const bf16x8& kf) {
  constexpr int aq = AQ_(QB, D0);
  if constexpr (D0 == 7) asm volatile("s_nop 1\n\tv_mfma_f32_32x32x16_bf16 %0, %1, a[%c2:%c3], %0\n\ts_nop 11" : "+v"(S) : "v"(kf), "i"(aq), "i"(aq + 3) : W4_ACLOB, W4_ACLOB_HI);
  else                   asm volatile("s_nop 1\n\tv_mfma_f32_32x32x16_bf16 %0, %1, a[%c2:%c3], %0" : "+v"(S) : "v"(kf), "i"(aq), "i"(aq + 3) : W4_ACLOB, W4_ACLOB_HI);
}
template <int QB, int DB> DI void tree_pv(const bf16x8& vf, const bf16x8& pk) {
  constexpr int ao = AO_(DB, QB);
  asm volatile("s_nop 1\n\tv_mfma_f32_32x32x16_bf16 a[%c2:%c3], %0, %1, a[%c2:%c3]" :: "v"(vf), "v"(pk), "i"(ao), "i"(ao + 15) : W4_ACLOB);
}
DI float bf16r(float x) { unsigned u = cvtpk(x, 0.f) & 0xFFFFu; return __uint_as_float(u << 16); }
DI float rec_bias(unsigned rec) {
  const float mv = __uint_as_float(rec & 0xFFFF0000u);
  return bf16r(bf16r(1.0f - mv) * -1.0e9f) * 1.4426950408889634f;
}
struct TreeSt {
  unsigned j, n0, ntt, cnt0, cnt1, g0;
  unsigned tkb, tvb, trb;
  __amdgpu_buffer_rsrc_t srdR, srdKt, srdVt;
};
DI unsigned bperm(unsigned srclane, unsigned v) { return (unsigned)__builtin_amdgcn_ds_bpermute((int)(srclane * 4u), (int)v); }
DI unsigned lane_id() { unsigned l; asm volatile("v_mbcnt_lo_u32_b32 %0, -1, 0\n\tv_mbcnt_hi_u32_b32 %0, -1, %0" : "=v"(l)); return l; }
DI void tree_rec_dma(const TreeSt& ts, unsigned i) {
  const unsigned l32 = lane_id() & 31u;
  const bool q1 = i >= ts.n0; const unsigned base = (q1 ? i - ts.n0 : i) * 32u + l32;
  unsigned off = ((ts.g0 + (q1 ? 1u : 0u)) * REC_CAP + base) * 4u;
  if (!(i < ts.ntt && base < (q1 ? ts.cnt1 : ts.cnt0))) off = 0xFFFFFFF0u;
  asm volatile("s_nop 4\n\ts_mov_b32 m0, %0\n\ts_nop 0\n\tbuffer_load_dword %1, %2, 0 offen lds" :: "s"(rfl(ts.trb + 256u * (i & 1u))), "v"(off), "s"(ts.srdR) : "m0", "memory");
}
DI unsigned tree_rec_read(const TreeSt& ts, unsigned i, unsigned lane) {
  unsigned r;
  asm volatile("ds_read_b32 %0, %1\n\ts_waitcnt lgkmcnt(0)" : "=v"(r) : "v"(ts.trb + 256u * (i & 1u) + 4u * (lane & 31u)) : "memory");
  return r;
}
DI void tree_issue(TreeSt& ts, unsigned i) {
  const unsigned lane = lane_id(), hi = lane >> 5;
  const unsigned rec = tree_rec_read(ts, i, lane);
  #pragma unroll
  for (int p = 0; p < 8; ++p) {
    const unsigned kk = bperm(4u * p + (lane >> 4), rec) & 0x7FFu;
    const unsigned ko = kk * 256u + 16u * ((lane & 15u) ^ ((4u * p + (lane >> 4)) & 7u));
    asm volatile("s_mov_b32 m0, %0\n\ts_nop 0\n\tbuffer_load_dwordx4 %1, %2, 0 offen lds" :: "s"(ts.tkb + 1024u * p), "v"(ko), "s"(ts.srdKt) : "m0", "memory");
  }
  #pragma unroll
  for (int g8 = 0; g8 < 4; ++g8) {
    const unsigned vk = bperm(8u * g8 + ((lane >> 2) & 7u), rec) & 0x7FFu;
    const unsigned vo = vk * 256u + 64u * hi + 16u * (lane & 3u);
    asm volatile("s_mov_b32 m0, %0\n\ts_nop 0\n\tbuffer_load_dwordx4 %1, %2, 0 offen lds" :: "s"(ts.tvb + 2048u * g8), "v"(vo), "s"(ts.srdVt) : "m0", "memory");
    asm volatile("s_mov_b32 m0, %0\n\ts_nop 0\n\tbuffer_load_dwordx4 %1, %2, %3 offen lds" :: "s"(ts.tvb + 2048u * g8 + 1024u), "v"(vo), "s"(ts.srdVt), "s"(128u) : "m0", "memory");
  }
}
template <int QB, int X, bool INLOOP> DI void tree_compute(W4C& c, const TreeSt& ts) {
  const unsigned lane = lane_id(), l32 = lane & 31u, hi = lane >> 5;
  const unsigned vkt = ts.tkb + 256u * l32;
  const unsigned vvt = ts.tvb + (((lane & 3u) << 3) | (((lane >> 2) & 3u) << 6) | (((lane >> 4) & 1u) << 5) | (hi << 8));
  f32x16 S;
  u32x4 r4[4];
  {
    const unsigned ra = ts.trb + 256u * (ts.j & 1u) + 16u * hi;
    asm volatile("ds_read_b128 %0, %4\n\tds_read_b128 %1, %4 offset:32\n\tds_read_b128 %2, %4 offset:64\n\tds_read_b128 %3, %4 offset:96\n\ts_waitcnt lgkmcnt(0)"
                 : "=&v"(r4[0]), "=&v"(r4[1]), "=&v"(r4[2]), "=&v"(r4[3]) : "v"(ra) : "memory");
  }
  #pragma unroll
  for (int g4 = 0; g4 < 4; ++g4) {
    #pragma unroll
    for (int k = 0; k < 4; ++k) {
      const unsigned rj = r4[g4][k];
      const bool own = ((rj >> 11) & 15u) == (l32 & 7u) + 1u;
      const float bv = rec_bias(rj) - c.mhat[QB];
      S[4 * g4 + k] = own ? bv : -1.0e30f;
    }
  }
  bf16x8 kf[8];
  #pragma unroll
  for (int d0 = 0; d0 < 8; ++d0) asm volatile("ds_read_b128 %0, %1 offset:%2" : "=v"(kf[d0]) : "v"(vkt + 16u * ((2u * (d0 & 3) + hi) ^ (l32 & 7u))), "i"(128 * (d0 >> 2)) : "memory");
  asm volatile("s_waitcnt lgkmcnt(0)" : "+v"(kf[0]), "+v"(kf[1]), "+v"(kf[2]), "+v"(kf[3]), "+v"(kf[4]), "+v"(kf[5]), "+v"(kf[6]), "+v"(kf[7]) :: "memory");
  tree_qk<QB, 0>(S, kf[0]); tree_qk<QB, 1>(S, kf[1]); tree_qk<QB, 2>(S, kf[2]); tree_qk<QB, 3>(S, kf[3]);
  tree_qk<QB, 4>(S, kf[4]); tree_qk<QB, 5>(S, kf[5]); tree_qk<QB, 6>(S, kf[6]); tree_qk<QB, 7>(S, kf[7]);
  float mx = max3f(S[0], S[1], S[2]);
  mx = max3f(mx, S[3], S[4]); mx = max3f(mx, S[5], S[6]); mx = max3f(mx, S[7], S[8]); mx = max3f(mx, S[9], S[10]);
  mx = max3f(mx, S[11], S[12]); mx = max3f(mx, S[13], S[14]); mx = max2f(mx, S[15]);
  { auto pr = __builtin_amdgcn_permlane32_swap(__float_as_uint(mx), __float_as_uint(mx), false, false);
    mx = max2f(__uint_as_float(pr[0]), __uint_as_float(pr[1])); }
  if (__builtin_expect(__any((int)(mx > c.THRv)), 0)) {
    const float dl = max2f(mx, 0.f), f = __builtin_amdgcn_exp2f(-dl);
    c.mhat[QB] = fadd_s(c.mhat[QB], dl);
    #pragma unroll
    for (int e = 0; e < 16; ++e) S[e] = fsub_s(S[e], dl);
    asm volatile("s_nop 7\n\ts_nop 7" ::: "memory");
    acc_scale_qb<QB>(f, make_iseq<64>{});
    asm volatile("s_nop 1" ::: "memory");
    c.lsum[QB] = fmul_s(c.lsum[QB], f);
    if constexpr (INLOOP) {
      #pragma unroll
      for (int e = 0; e < 16; ++e) { c.S[X][0][QB][e] = fmul_s(c.S[X][0][QB][e], f); c.S[X][1][QB][e] = fsub_s(c.S[X][1][QB][e], dl); }
      c.rs0[QB] = fmul_s(c.rs0[QB], f); c.rs1[QB] = fmul_s(c.rs1[QB], f);
    }
    negm_bcast(c.NEGM[QB], w4_aone(), -c.mhat[QB]);
  }
  float rs = 0.f;
  #pragma unroll
  for (int e = 0; e < 16; ++e) { S[e] = __builtin_amdgcn_exp2f(S[e]); rs += S[e]; }
  { auto pr = __builtin_amdgcn_permlane32_swap(__float_as_uint(rs), __float_as_uint(rs), false, false);
    c.lsum[QB] = c.lsum[QB] + __uint_as_float(pr[0]) + __uint_as_float(pr[1]); }
  bf16x8 pk[2];
  #pragma unroll
  for (int ks = 0; ks < 2; ++ks) { u32x4 w;
    #pragma unroll
    for (int p = 0; p < 4; ++p) w[p] = cvtpk(S[8 * ks + 2 * p], S[8 * ks + 2 * p + 1]);
    pk[ks] = __builtin_bit_cast(bf16x8, w); }
  #pragma unroll
  for (int ks = 0; ks < 2; ++ks) {
    bf16x4 lo[4], up[4];
    #pragma unroll
    for (int db = 0; db < 4; ++db) {
      asm volatile("ds_read_b64_tr_b16 %0, %1 offset:%2" : "=v"(lo[db]) : "v"(vvt), "i"(4096 * ks + 512 * db) : "memory");
      asm volatile("ds_read_b64_tr_b16 %0, %1 offset:%2" : "=v"(up[db]) : "v"(vvt), "i"(4096 * ks + 2048 + 512 * db) : "memory");
    }
    asm volatile("s_waitcnt lgkmcnt(0)" : "+v"(lo[0]), "+v"(lo[1]), "+v"(lo[2]), "+v"(lo[3]), "+v"(up[0]), "+v"(up[1]), "+v"(up[2]), "+v"(up[3]) :: "memory");
    tree_pv<QB, 0>(__builtin_shufflevector(lo[0], up[0], 0, 1, 2, 3, 4, 5, 6, 7), pk[ks]);
    tree_pv<QB, 1>(__builtin_shufflevector(lo[1], up[1], 0, 1, 2, 3, 4, 5, 6, 7), pk[ks]);
    tree_pv<QB, 2>(__builtin_shufflevector(lo[2], up[2], 0, 1, 2, 3, 4, 5, 6, 7), pk[ks]);
    tree_pv<QB, 3>(__builtin_shufflevector(lo[3], up[3], 0, 1, 2, 3, 4, 5, 6, 7), pk[ks]);
  }
  asm volatile("s_nop 1" ::: "memory");
}
template <int X, bool INLOOP> DI void tree_block(W4C& c, TreeSt& ts) {
  if (ts.j < ts.ntt) {
    if constexpr (!INLOOP) asm volatile("s_waitcnt vmcnt(0)" ::: "memory");
    if (ts.j < ts.n0) tree_compute<0, X, INLOOP>(c, ts); else tree_compute<1, X, INLOOP>(c, ts);
    ts.j += 1u;
    if (ts.j < ts.ntt) tree_issue(ts, ts.j);
    tree_rec_dma(ts, ts.j + 1u);
  }
}
template <int R> DI void acc_zero1() { asm volatile("v_accvgpr_write_b32 a[%c0], 0" :: "i"(R) : W4_ACLOB); }
template <int... Rs> DI void acc_zero(iseq<Rs...>) { (acc_zero1<Rs>(), ...); }

extern "C" __global__ __launch_bounds__(256, 1)
void tree_attn_main(const unsigned short* __restrict__ Q, const unsigned short* __restrict__ Kc, const unsigned short* __restrict__ Vc,
                    const unsigned short* __restrict__ Ktr, const unsigned short* __restrict__ Vtr,
                    const unsigned* __restrict__ cnts, const unsigned* __restrict__ recs, unsigned short* __restrict__ O) {
  __shared__ __attribute__((aligned(16))) char lds[LDS_BYTES];
  const unsigned tid = threadIdx.x, lane = tid & 63u, wid = tid >> 6, l32 = lane & 31u, hi = lane >> 5;
  const float CSC = 0.12751743082f;
  W4C c;
  c.THRv = THR;
  c.ldsb = (unsigned)(size_t)lds;
  #pragma unroll
  for (int j = 0; j < 4; ++j) c.vkb[j] = c.ldsb + KRING + 256u * l32 + 16u * ((2u * j + hi) ^ (l32 & 7u));
  c.vvb = c.ldsb + VRING + (((lane & 3u) << 3) | (((lane >> 2) & 3u) << 6) | (((lane >> 4) & 1u) << 5) | (hi << 8));
  #pragma unroll
  for (int par = 0; par < 2; ++par) c.vksrc[par] = KRS * (lane >> 4) + 16u * ((lane & 15u) ^ (4u * par + (lane >> 4)));
  c.vvsrc = KRS * ((lane >> 2) & 7u) + 64u * hi + 16u * (lane & 3u);
  c.m0k = rfl(c.ldsb + KRING + 4096u * wid); c.m0v = rfl(c.ldsb + VRING + 4096u * wid);
  c.sofk = rfl(KRS * 16u * wid); c.sofv = rfl(KRS * 16u * wid);

  const unsigned blk = blockIdx.x, kvh = blk & 7u, pblk = blk >> 3;
  const unsigned P0 = 64u * pblk + 16u * wid;
  const char* Kbase = (const char*)Kc + (size_t)kvh * C * 256;
  const char* Vbase = (const char*)Vc + (size_t)kvh * C * 256;
  c.srdK = __builtin_amdgcn_make_buffer_rsrc((void*)Kbase, (short)0, C * 256, SRD_WORD3);
  c.srdV = __builtin_amdgcn_make_buffer_rsrc((void*)Vbase, (short)0, C * 256, SRD_WORD3);
  auto dma_k = [&](int tk) {
    const unsigned slot = (unsigned)(tk & 1) * 0x4000u, skv = KTILEB * (unsigned)tk;
    #pragma unroll
    for (int q = 0; q < 4; ++q)
      asm volatile("s_nop 4\n\ts_mov_b32 m0, %0\n\ts_nop 0\n\tbuffer_load_dwordx4 %1, %2, %3 offen lds"
                   :: "s"(rfl(c.m0k + slot + 1024u * q)), "v"(c.vksrc[q & 1]), "s"(c.srdK), "s"(rfl(skv + c.sofk + KRS * 4u * q)) : "m0", "memory");
  };
  auto dma_v = [&](int tv) {
    const unsigned slot = (unsigned)(tv & 1) * 0x4000u, skv = KTILEB * (unsigned)tv;
    #pragma unroll
    for (int q = 0; q < 4; ++q)
      asm volatile("s_nop 4\n\ts_mov_b32 m0, %0\n\ts_nop 0\n\tbuffer_load_dwordx4 %1, %2, %3 offen lds"
                   :: "s"(rfl(c.m0v + slot + 1024u * q)), "v"(c.vvsrc), "s"(c.srdV), "s"(rfl(skv + c.sofv + KRS * 8u * (q >> 1) + 128u * (q & 1))) : "m0", "memory");
  };
  const unsigned qstg = rfl(c.ldsb + TREEV + 16384u * wid);
  {
    const __amdgpu_buffer_rsrc_t srdQ = __builtin_amdgcn_make_buffer_rsrc((void*)Q, (short)0, HQ * T * 256, SRD_WORD3);
    #pragma unroll
    for (int qp = 0; qp < 16; ++qp)
      asm volatile("s_nop 4\n\ts_mov_b32 m0, %0\n\ts_nop 0\n\tbuffer_load_dwordx4 %1, %2, %3 offen lds"
                   :: "s"(rfl(qstg + 1024u * qp)), "v"(c.vksrc[qp & 1]), "s"(srdQ), "s"(rfl(((4u * kvh + ((qp >> 1) & 3)) * T + P0 + 8u * (qp >> 3) + 4u * (qp & 1)) * 256u)) : "m0", "memory");
  }
  dma_k(0); dma_v(0); dma_k(1);
  acc_zero(make_iseq<128>{});
  asm volatile("s_waitcnt vmcnt(12)" ::: "memory");
  {
    #pragma unroll
    for (int qb = 0; qb < 2; ++qb) {
      bf16x8 qf[8];
      #pragma unroll
      for (int d0 = 0; d0 < 8; ++d0) asm volatile("ds_read_b128 %0, %1 offset:%2" : "=v"(qf[d0]) : "v"(qstg + 256u * l32 + 16u * ((2u * (d0 & 3) + hi) ^ (l32 & 7u))), "i"(8192 * qb + 128 * (d0 >> 2)) : "memory");
      asm volatile("s_waitcnt lgkmcnt(0)" : "+v"(qf[0]), "+v"(qf[1]), "+v"(qf[2]), "+v"(qf[3]), "+v"(qf[4]), "+v"(qf[5]), "+v"(qf[6]), "+v"(qf[7]) :: "memory");
      u32x4 w;
      #define QW(I) { const bf16x8 q8 = qf[I]; _Pragma("unroll") for (int e = 0; e < 4; ++e) w[e] = cvtpk(fmul_s((float)q8[2 * e], CSC), fmul_s((float)q8[2 * e + 1], CSC)); if (qb == 0) q_write1<I>(w); else q_write1<8 + I>(w); }
      QW(0) QW(1) QW(2) QW(3) QW(4) QW(5) QW(6) QW(7)
      #undef QW
    }
  }
  TreeSt ts;
  ts.g0 = P0 >> 3;
  ts.srdR = __builtin_amdgcn_make_buffer_rsrc((void*)recs, (short)0, 256u * REC_CAP * 4u, SRD_WORD3);
  ts.cnt0 = cnts[ts.g0]; ts.cnt1 = cnts[ts.g0 + 1u];
  ts.n0 = (ts.cnt0 + 31u) >> 5; ts.ntt = ts.n0 + ((ts.cnt1 + 31u) >> 5); ts.j = 0u;
  ts.srdKt = __builtin_amdgcn_make_buffer_rsrc((void*)((const char*)Ktr + (size_t)kvh * T * 256), (short)0, T * 256, SRD_WORD3);
  ts.srdVt = __builtin_amdgcn_make_buffer_rsrc((void*)((const char*)Vtr + (size_t)kvh * T * 256), (short)0, T * 256, SRD_WORD3);
  ts.tkb = rfl(c.ldsb + TREEV + 16384u * wid); ts.tvb = ts.tkb + 8192u;
  ts.trb = rfl(c.ldsb + TREER + 512u * wid);
  tree_rec_dma(ts, 0u); tree_rec_dma(ts, 1u);
  c.mhat[0] = c.mhat[1] = 0.f; c.lsum[0] = c.lsum[1] = 0.f; c.fct[0] = c.fct[1] = 1.f; c.pend = false;
  asm volatile("s_waitcnt vmcnt(0)\n\ts_barrier" ::: "memory");
  k_reads(c, 0u, make_iseq<16>{});
  if (0u < ts.ntt) tree_issue(ts, 0u);
  asm volatile("s_waitcnt lgkmcnt(0)\n\ts_nop 1" ::: "memory");

  phaseA<0, true>(c, 0);  phaseB<0, true,  false>(c, 0);
  phaseA<1, false>(c, 1); phaseB<1, false, false>(c, 1); tree_block<1, true>(c, ts);
  for (int t = 2; t < NT - 2; t += 2) {
    phaseA<0, false>(c, t);     phaseB<0, false, false>(c, t);     tree_block<0, true>(c, ts);
    phaseA<1, false>(c, t + 1); phaseB<1, false, false>(c, t + 1); tree_block<1, true>(c, ts);
  }
  phaseA<0, false>(c, NT - 2); phaseB<0, false, false,  false,  true,  true>(c, NT - 2);
  phaseA<1, false>(c, NT - 1); phaseB<1, false, false,  false,  false,  false>(c, NT - 1);
  {
    const unsigned vslot = (unsigned)((NT - 1) & 1) * 0x4000u;
    tr_reads(c, vslot, make_iseq<32>{});
    #pragma unroll
    for (int e = 0; e < 32; ++e) c.S[1][1][e >> 4][e & 15] = __builtin_amdgcn_exp2f(c.S[1][1][e >> 4][e & 15]);
    packs<1>(c, make_iseq<32>{});
    rowsums<1>(c, iseq<8, 9, 10, 11, 12, 13, 14, 15, 24, 25, 26, 27, 28, 29, 30, 31>{});
    #pragma unroll
    for (int qb = 0; qb < 2; ++qb) { const float rs = fadd_s(c.rs0[qb], c.rs1[qb]);
      auto pr = __builtin_amdgcn_permlane32_swap(__float_as_uint(rs), __float_as_uint(rs), false, false);
      c.lsum[qb] = fadd_s(c.lsum[qb], fadd_s(__uint_as_float(pr[0]), __uint_as_float(pr[1]))); }
    asm volatile("s_waitcnt lgkmcnt(0)" : W4_VLO :: "memory"); asm volatile("" : W4_VUP :: "memory");
    GAP();
    #pragma unroll
    for (int ks = 0; ks < 4; ++ks)
      #pragma unroll
      for (int db = 0; db < 4; ++db) c.Vf[db][ks] = __builtin_shufflevector(c.vlo[db][ks], c.vup[db][ks], 0, 1, 2, 3, 4, 5, 6, 7);
    asm volatile("s_nop 1" ::: "memory");
    pv_drain(c, make_iseq<32>{});
    asm volatile("s_nop 7\n\ts_nop 7" ::: "memory");
  }
  while (ts.j < ts.ntt) tree_block<0, false>(c, ts);
  asm volatile("s_barrier" ::: "memory");
  {
    char* stg = lds + 16384u * wid;
    #pragma unroll
    for (int qb = 0; qb < 2; ++qb) {
      const float il = c.lsum[qb] > 0.f ? __builtin_amdgcn_rcpf(c.lsum[qb]) : 0.f;
      #define EPI_DB(DB) { float o[16]; if (qb == 0) acc_read16<DB, 0>(o, make_iseq<16>{}); else acc_read16<DB, 1>(o, make_iseq<16>{}); \
        _Pragma("unroll") for (int g = 0; g < 4; ++g) { u32x2 w; w[0] = cvtpk(o[4 * g] * il, o[4 * g + 1] * il); w[1] = cvtpk(o[4 * g + 2] * il, o[4 * g + 3] * il); \
          *(u32x2*)(stg + 32u * 256u * qb + l32 * 256u + 16u * ((4u * DB + g) ^ (l32 & 15u)) + 8u * hi) = w; } }
      EPI_DB(0) EPI_DB(1) EPI_DB(2) EPI_DB(3)
      #undef EPI_DB
    }
    asm volatile("s_waitcnt lgkmcnt(0)" ::: "memory");
    #pragma unroll
    for (int j = 0; j < 16; ++j) {
      const unsigned r = 4u * j + (lane >> 4), cc = lane & 15u;
      const u32x4 v = *(const u32x4*)(stg + r * 256u + 16u * (cc ^ (r & 15u)));
      char* orow = (char*)O + ((size_t)(4u * kvh + ((r >> 3) & 3u)) * T + P0 + 8u * (r >> 5) + (r & 7u)) * 256u + 16u * cc;
      *(u32x4*)orow = v;
    }
  }
}

__global__ __launch_bounds__(256) void tree_prepass(const u32x4* __restrict__ mask, unsigned* __restrict__ cnts, unsigned* __restrict__ recs,
                                                    const char* __restrict__ Q, const char* __restrict__ Kc, const char* __restrict__ Vc, const char* __restrict__ Kt, const char* __restrict__ Vt) {
  __shared__ unsigned wcnt[4];
  const unsigned g = blockIdx.x, tid = threadIdx.x, lane = tid & 63u, wid = tid >> 6;
  unsigned warm = 0;
  {
    const unsigned kvh = g & 7u, i = (g >> 3) * 32u + (tid & 31u);
    if (tid < 32u) {
      const char* p = i < 512u ? Q + (size_t)kvh * 4u * T * 256u + (size_t)i * 4096u
                    : (i < 640u ? Kc : i < 768u ? Vc : i < 896u ? Kt : Vt) + (size_t)kvh * T * 256u + (size_t)(i & 127u) * 4096u;
      warm = *(const unsigned*)p;
    }
  }
  u32x4 v[8];
  #pragma unroll
  for (int r = 0; r < 8; ++r) v[r] = mask[(size_t)(8u * g + r) * 256u + tid];
  unsigned nz = 0;
  #pragma unroll
  for (int r = 0; r < 8; ++r)
    #pragma unroll
    for (int k = 0; k < 4; ++k) nz += ((v[r][k] & 0x7FFFu) != 0u) + ((v[r][k] & 0x7FFF0000u) != 0u);
  unsigned tot = 0;
  if (__any(nz != 0u)) {
    #pragma unroll
    for (int o = 32; o >= 1; o >>= 1) nz += __shfl_xor(nz, o);
    tot = nz;
  }
  if (lane == 0) wcnt[wid] = tot;
  __syncthreads();
  unsigned base = 0;
  for (unsigned w = 0; w < wid; ++w) base += wcnt[w];
  if (tid == 0) cnts[g] = wcnt[0] + wcnt[1] + wcnt[2] + wcnt[3];
  asm volatile("" :: "v"(warm));
  if (tot == 0) return;
  unsigned* rg = recs + (size_t)g * REC_CAP;
  #pragma unroll
  for (int r = 0; r < 8; ++r) {
    if (!__any((v[r][0] | v[r][1] | v[r][2] | v[r][3]) & 0x7FFF7FFFu)) continue;
    #pragma unroll
    for (int e = 0; e < 8; ++e) {
      const unsigned bits = (v[r][e >> 1] >> (16 * (e & 1))) & 0xFFFFu;
      const bool on = (bits & 0x7FFFu) != 0u;
      const unsigned long long m = __ballot(on);
      if (on) rg[base + __popcll(m & ((1ull << lane) - 1ull))] = (8u * tid + e) | ((unsigned)(r + 1) << 11) | (bits << 16);
      base += __popcll(m);
    }
  }
}

extern "C" void kernel_launch(void* const* d_in, const int* in_sizes, int n_in, void* d_out, int out_size, void* d_ws, size_t ws_size, hipStream_t stream) {
  unsigned* cnts = (unsigned*)d_ws;
  unsigned* recs = (unsigned*)((char*)d_ws + 4096);
  hipLaunchKernelGGL(tree_prepass, dim3(256), dim3(256), 0, stream, (const u32x4*)d_in[5], cnts, recs, (const char*)d_in[0], (const char*)d_in[1], (const char*)d_in[2], (const char*)d_in[3], (const char*)d_in[4]);
  hipLaunchKernelGGL(tree_attn_main, dim3(256), dim3(256), 0, stream, (const unsigned short*)d_in[0], (const unsigned short*)d_in[1], (const unsigned short*)d_in[2],
                     (const unsigned short*)d_in[3], (const unsigned short*)d_in[4], (const unsigned*)cnts, (const unsigned*)recs, (unsigned short*)d_out);
}
```
